```python
import torch

def _softmax(x, axis=-1):
    return torch.softmax(x, dim=axis)
def _gelu(x):
    return torch.nn.functional.gelu(x, approximate="tanh")
def _silu(x):
    return torch.nn.functional.silu(x)
def _sigmoid(x):
    return torch.sigmoid(x)
def _layernorm(x, axis=-1, eps=1e-5):
    m = x.mean(dim=axis, keepdim=True)
    v = x.var(dim=axis, keepdim=True, unbiased=False)
    return (x - m) / torch.sqrt(v + eps)
def _scatter_add(tgt, idx, src, axis=0):
    return tgt.clone().index_add_(axis, idx, src)
def _segment_sum(src, seg, n):
    out = torch.zeros((n,) + src.shape[1:], dtype=src.dtype)
    return out.index_add_(0, seg, src)
def _pad(x, pw):
    flat = tuple(v for pair in reversed(pw) for v in pair)
    return torch.nn.functional.pad(x, flat)

kernel_name = 'gated_mix__ssm_conv_s5__glu_gated_swiglu__prompt'

def setup_inputs():

    _w = (lambda a: (lambda t: t.to(torch.bfloat16) if t.is_floating_point() else t)(torch.as_tensor(a)))
    def _A_setup():
        B, S, D = 1, 2048, 448
        P = D
        Lambda_re = -0.5 * (lambda _x: torch.exp(torch.as_tensor(_x)))((lambda *s: torch.randn(*s, dtype=torch.bfloat16) * 0.2)(P))
        Lambda_im = (lambda *s: torch.randn(*s, dtype=torch.bfloat16) * 0.4)(P) * 1.0
        B_re = (lambda *s: torch.randn(*s, dtype=torch.bfloat16) * 0.2)(P, D) * (D ** -0.5)
        B_im = (lambda *s: torch.randn(*s, dtype=torch.bfloat16) * 0.2)(P, D) * (D ** -0.5)
        C_re = (lambda *s: torch.randn(*s, dtype=torch.bfloat16) * 0.2)(D, P) * (P ** -0.5)
        C_im = (lambda *s: torch.randn(*s, dtype=torch.bfloat16) * 0.2)(D, P) * (P ** -0.5)
        log_dt = (lambda *s: torch.randn(*s, dtype=torch.bfloat16) * 0.2)(P) * 0.3 - 2.0
        Dskip = (lambda *s: torch.randn(*s, dtype=torch.bfloat16) * 0.05)(D)
        return {
            "x": _w((lambda *s: torch.randn(*s, dtype=torch.bfloat16) * 0.05)(B, S, D)),
            "Lambda_re": _w(Lambda_re),
            "Lambda_im": _w(Lambda_im),
            "B_re": _w(B_re),
            "B_im": _w(B_im),
            "C_re": _w(C_re),
            "C_im": _w(C_im),
            "log_dt": _w(log_dt),
            "Dskip": _w(Dskip),
        }
    _A_in = _A_setup()

    def _B_setup():
        B, S, D, F = 1, 2048, 448, 1792
        return {
            "x": _w((lambda *s: torch.randn(*s, dtype=torch.bfloat16) * 0.05)(B, S, D)),
            "w1": _w((lambda *s: torch.randn(*s, dtype=torch.bfloat16) * 0.8)(D, F) * (2.0 / D) ** 0.5),
            "w3": _w((lambda *s: torch.randn(*s, dtype=torch.bfloat16) * 0.8)(D, F) * (2.0 / D) ** 0.5),
            "w2": _w((lambda *s: torch.randn(*s, dtype=torch.bfloat16) * 0.8)(F, D) * (2.0 / F) ** 0.5),
        }
    _B_in = _B_setup()

    return {
        "A_x": _A_in["x"],
        "A_Lambda_re": _A_in["Lambda_re"],
        "A_Lambda_im": _A_in["Lambda_im"],
        "A_B_re": _A_in["B_re"],
        "A_B_im": _A_in["B_im"],
        "A_C_re": _A_in["C_re"],
        "A_C_im": _A_in["C_im"],
        "A_log_dt": _A_in["log_dt"],
        "A_Dskip": _A_in["Dskip"],
        "B_x": _B_in["x"],
        "B_w1": _B_in["w1"],
        "B_w3": _B_in["w3"],
        "B_w2": _B_in["w2"],
        "gate_w": _w((lambda *s: torch.randn(*s, dtype=torch.bfloat16) * 0.05)(448) * 16.0),
    }

def reference(A_x, A_Lambda_re, A_Lambda_im, A_B_re, A_B_im, A_C_re, A_C_im, A_log_dt, A_Dskip, B_x, B_w1, B_w3, B_w2, gate_w):
    def _A_ref(x, Lambda_re, Lambda_im, B_re, B_im, C_re, C_im, log_dt, Dskip):
        Bsz, L, H = x.shape
        Lam = Lambda_re + 1j * Lambda_im
        Bmat = B_re + 1j * B_im
        Cmat = C_re + 1j * C_im
        dt = torch.exp(log_dt)
        Lam_bar = torch.exp(dt * Lam)
        B_bar = ((Lam_bar - 1.0) / Lam)[:, None] * Bmat
        Bu = torch.einsum("bld,pd->blp", x + 0j, B_bar)
        h = torch.zeros_like(Bu[:, 0, :])
        hs = []
        for t in range(L):
            h = Lam_bar[None, :] * h + Bu[:, t, :]
            hs.append(h)
        hseq = torch.stack(hs, 1)
        y = 2.0 * torch.einsum("blp,dp->bld", hseq, Cmat).real + x * Dskip[None, None, :]
        return y

    def _B_ref(x, w1, w3, w2):
        gate = torch.einsum("bsd,df->bsf", x, w1)
        up = torch.einsum("bsd,df->bsf", x, w3)
        h = _silu(gate) * up
        out = torch.einsum("bsf,fd->bsd", h, w2)
        return out

    x = A_x
    g = _sigmoid((x * gate_w).sum(-1, keepdims=True))
    a = _A_ref(x, A_Lambda_re, A_Lambda_im, A_B_re, A_B_im, A_C_re, A_C_im, A_log_dt, A_Dskip)
    b = _B_ref(x, B_w1, B_w3, B_w2)
    return g * a + (1 - g) * b
```

```cpp
#include <hip/hip_runtime.h>
#include <hip/hip_bf16.h>
#include <stdint.h>
typedef unsigned short u16;
static __device__ __forceinline__ float bf2f(u16 v){ return __uint_as_float(((unsigned)v)<<16); }
static __device__ __forceinline__ float rbf(float f){ unsigned u=__float_as_uint(f); u += 0x7FFFu + ((u>>16)&1u); return __uint_as_float(u & 0xFFFF0000u); }
constexpr int L=2048, D=448, P=448, F=1792;

__global__ void k_bu(const u16* x, const u16* Bre, const u16* Bim, const u16* lre, const u16* lim, const u16* ldt, float2* bu){
  int p = blockIdx.x*64 + threadIdx.x; int t = blockIdx.y;
  float sr=0, si=0;
  for(int d=0; d<D; ++d){ float xv=bf2f(x[t*D+d]); sr += xv*bf2f(Bre[p*D+d]); si += xv*bf2f(Bim[p*D+d]); }
  float dt = rbf(expf(bf2f(ldt[p])));
  float a=bf2f(lre[p]), b=bf2f(lim[p]);
  float m=expf(dt*a); float lr=m*cosf(dt*b), li=m*sinf(dt*b);
  float nr=lr-1.f, ni=li; float den=a*a+b*b;
  float cr=(nr*a+ni*b)/den, ci=(ni*a-nr*b)/den;
  bu[t*P+p]=make_float2(cr*sr-ci*si, cr*si+ci*sr);
}
__global__ void k_scan(const u16* lre, const u16* lim, const u16* ldt, float2* bu){
  int p = blockIdx.x*64 + threadIdx.x;
  float dt = rbf(expf(bf2f(ldt[p])));
  float a=bf2f(lre[p]), b=bf2f(lim[p]);
  float m=expf(dt*a); float lr=m*cosf(dt*b), li=m*sinf(dt*b);
  float hr=0,hi=0;
  for(int t=0;t<L;++t){ float2 u=bu[t*P+p]; float nr=lr*hr-li*hi+u.x, ni=lr*hi+li*hr+u.y; hr=nr;hi=ni; bu[t*P+p]=make_float2(hr,hi);}
}
__global__ void k_h(const u16* x, const u16* w1, const u16* w3, float* h){
  int f = blockIdx.x*64 + threadIdx.x; int t = blockIdx.y;
  float g=0,u=0;
  for(int d=0; d<D; ++d){ float xv=bf2f(x[t*D+d]); g += xv*bf2f(w1[d*F+f]); u += xv*bf2f(w3[d*F+f]); }
  h[t*F+f] = rbf(g*u/(1.f+expf(-g)));
}
__global__ void k_out(const u16* x, const float2* hs, const float* h, const u16* Cre, const u16* Cim, const u16* w2, const u16* dsk, const u16* gw, float* out){
  int d = blockIdx.x*64 + threadIdx.x; int t = blockIdx.y;
  float y=0;
  for(int p=0;p<P;++p){ float2 v=hs[t*P+p]; y += rbf(v.x)*bf2f(Cre[d*P+p]) - rbf(v.y)*bf2f(Cim[d*P+p]); }
  float b=0;
  for(int f=0;f<F;++f) b += h[t*F+f]*bf2f(w2[f*D+d]);
  float s=0;
  for(int k=0;k<D;++k) s += bf2f(x[t*D+k])*bf2f(gw[k]);
  float g = 1.f/(1.f+expf(-s));
  float a = 2.f*y + bf2f(x[t*D+d])*bf2f(dsk[d]);
  out[t*D+d] = g*a + (1.f-g)*b;
}
extern "C" void kernel_launch(void* const* d_in, const int* in_sizes, int n_in, void* d_out, int out_size, void* d_ws, size_t ws_size, hipStream_t stream){
  const u16* x=(const u16*)d_in[0]; const u16* lre=(const u16*)d_in[1]; const u16* lim=(const u16*)d_in[2];
  const u16* Bre=(const u16*)d_in[3]; const u16* Bim=(const u16*)d_in[4]; const u16* Cre=(const u16*)d_in[5]; const u16* Cim=(const u16*)d_in[6];
  const u16* ldt=(const u16*)d_in[7]; const u16* dsk=(const u16*)d_in[8];
  const u16* w1=(const u16*)d_in[10]; const u16* w3=(const u16*)d_in[11]; const u16* w2=(const u16*)d_in[12]; const u16* gw=(const u16*)d_in[13];
  float2* bu=(float2*)d_ws; float* h=(float*)((char*)d_ws + (size_t)L*P*8);
  k_bu<<<dim3(P/64,L),64,0,stream>>>(x,Bre,Bim,lre,lim,ldt,bu);
  k_scan<<<P/64,64,0,stream>>>(lre,lim,ldt,bu);
  k_h<<<dim3(F/64,L),64,0,stream>>>(x,w1,w3,h);
  k_out<<<dim3(D/64,L),64,0,stream>>>(x,bu,h,Cre,Cim,w2,dsk,gw,(float*)d_out);
}
```

```cpp
#include <hip/hip_runtime.h>
#include <stdint.h>

typedef unsigned short u16;
typedef unsigned int u32;
typedef unsigned long long u64;
typedef short s16x4 __attribute__((ext_vector_type(4)));
typedef short s16x8 __attribute__((ext_vector_type(8)));
typedef __bf16 bf16x8 __attribute__((ext_vector_type(8)));
typedef __bf16 bf16x2 __attribute__((ext_vector_type(2)));
typedef float f32x2 __attribute__((ext_vector_type(2)));
typedef float f32x4 __attribute__((ext_vector_type(4)));
typedef float f32x16 __attribute__((ext_vector_type(16)));
typedef u32 u32x2 __attribute__((ext_vector_type(2)));
typedef u32 u32x4 __attribute__((ext_vector_type(4)));
#define LAS __attribute__((address_space(3)))
#define GAS __attribute__((address_space(1)))
#define RLX_AGENT __ATOMIC_RELAXED, __HIP_MEMORY_SCOPE_AGENT

constexpr int L = 2048, D = 448, F = 1792;
constexpr int KS2 = 168;
constexpr size_t WS_H = 0;
constexpr size_t WS_WT = WS_H + (size_t)64 * KS2 * 1024;
constexpr size_t WS_G = WS_WT + (size_t)14 * KS2 * 1024;
constexpr size_t WS_BASE = WS_G + 8192;
constexpr size_t WS_GRAN = WS_BASE + 28 * 128;
constexpr size_t WS_END = WS_GRAN + (size_t)28 * 64 * 256;
constexpr size_t WS_STAMP = (WS_END + 4095) & ~(size_t)4095;

struct Params {
  const u16 *x, *lre, *lim, *bre, *bim, *cre, *cim, *ldt, *dsk, *w1, *w3, *w2, *gw;
  float* out;
  char* ws;
};

static __device__ __forceinline__ float bf2f(u32 v) { return __uint_as_float(v << 16); }
static __device__ __forceinline__ float bflo(u32 v) { return __uint_as_float(v << 16); }
static __device__ __forceinline__ float bfhi(u32 v) { return __uint_as_float(v & 0xFFFF0000u); }
static __device__ __forceinline__ u32 pk2(float a, float b) {
  f32x2 f = {a, b};
  return __builtin_bit_cast(u32, __builtin_convertvector(f, bf16x2));
}
static __device__ __forceinline__ f32x16 mfma(u32x4 a, u32x4 b, f32x16 c) {
  return __builtin_amdgcn_mfma_f32_32x32x16_bf16(__builtin_bit_cast(bf16x8, a), __builtin_bit_cast(bf16x8, b), c, 0, 0, 0);
}
static __device__ __forceinline__ void glds16(const void* g, LAS char* l) {
  __builtin_amdgcn_global_load_lds((const GAS void*)g, (LAS void*)l, 16, 0, 0);
}
#define WAIT_V(n) asm volatile("s_waitcnt vmcnt(" #n ")" ::: "memory")
#define WAIT_L0() asm volatile("s_waitcnt lgkmcnt(0)" ::: "memory")
#define BAR() __builtin_amdgcn_s_barrier()
#define SCHED() __builtin_amdgcn_sched_barrier(0)

#define STAMP_AT(kern, slot) do {} while (0)

struct cpx { float r, i; };
static __device__ __forceinline__ cpx cmul(cpx a, cpx b) { return {a.r * b.r - a.i * b.i, a.r * b.i + a.i * b.r}; }
static __device__ __forceinline__ cpx cfma(cpx a, cpx b, cpx c) {
  return {fmaf(a.r, b.r, fmaf(-a.i, b.i, c.r)), fmaf(a.r, b.i, fmaf(a.i, b.r, c.i))};
}

constexpr int STAGE = 53248, OFF_W1 = 32768, OFF_W3 = 40960, OFF_BW = 49152, OFF_AGG = 3 * STAGE;

static __device__ __forceinline__ void spare_job(const Params& p, int sidx) {
  const int tid = threadIdx.x, lane = tid & 63, w = tid >> 6;
  const int gt = sidx * 512 + tid;
  char* WT = p.ws + WS_WT;
  u32x4 xr[8];
  const int lc = lane < 56 ? lane : 55;
#pragma unroll
  for (int i = 0; i < 8; ++i) xr[i] = *(const u32x4*)(p.x + (size_t)(sidx * 64 + w * 8 + i) * D + lc * 8);
  const u32x4 gwv = *(const u32x4*)(p.gw + lc * 8);
  u32x4 a[8];
  const int blk = gt < 12544 ? gt : 12543;
  const int kb = blk / 56, cg = blk % 56;
#pragma unroll
  for (int j = 0; j < 8; ++j) a[j] = *(const u32x4*)(p.w2 + (size_t)(kb * 8 + j) * D + cg * 8);
  u32x4 cr[2], ci[2];
#pragma unroll
  for (int it = 0; it < 2; ++it) {
    int item = gt + it * 16384; item = item < 25088 ? item : 25087;
    cr[it] = *(const u32x4*)(p.cre + (size_t)item * 8);
    ci[it] = *(const u32x4*)(p.cim + (size_t)item * 8);
  }
  if (gt < 12544) {
    const int ks = kb >> 1, hk = kb & 1, nb = cg >> 2, c0 = (cg & 3) * 8;
    char* dst = WT + ((size_t)(nb * KS2 + ks) * 64 + hk * 32 + c0) * 16;
#pragma unroll
    for (int i = 0; i < 8; ++i) {
      u32x4 o;
#pragma unroll
      for (int j = 0; j < 4; ++j) {
        const u32 lo = a[2 * j][i >> 1], hi = a[2 * j + 1][i >> 1];
        o[j] = (i & 1) ? ((lo >> 16) | (hi & 0xFFFF0000u)) : ((lo & 0xFFFFu) | (hi << 16));
      }
      *(u32x4*)(dst + i * 16) = o;
    }
  }
#pragma unroll
  for (int it = 0; it < 2; ++it) {
    const int item = gt + it * 16384;
    if (item < 25088) {
      const int n = item / 56, pg = item % 56;
      char* dst = WT + ((size_t)((n >> 5) * KS2 + 112 + pg) * 64 + (n & 31)) * 16;
#pragma unroll
      for (int hk = 0; hk < 2; ++hk) {
        u32x4 o;
#pragma unroll
        for (int j = 0; j < 2; ++j) {
          const u32 r = cr[it][2 * hk + j], m = ci[it][2 * hk + j] ^ 0x80008000u;
          o[2 * j] = (r & 0xFFFFu) | (m << 16);
          o[2 * j + 1] = (r >> 16) | (m & 0xFFFF0000u);
        }
        *(u32x4*)(dst + hk * 512) = o;
      }
    }
  }
#pragma unroll
  for (int i = 0; i < 8; ++i) {
    float s = 0.f;
#pragma unroll
    for (int j = 0; j < 4; ++j) s += bflo(xr[i][j]) * bflo(gwv[j]) + bfhi(xr[i][j]) * bfhi(gwv[j]);
    if (lane >= 56) s = 0.f;
#pragma unroll
    for (int o = 32; o > 0; o >>= 1) s += __shfl_xor(s, o);
    if (lane == 0) ((float*)(p.ws + WS_G))[sidx * 64 + w * 8 + i] = 1.f / (1.f + __expf(-s));
  }
}

struct Frags { u32x4 x, b; u32x2 w[4][2]; };

__global__ __launch_bounds__(512, 2) void k1(Params p) {
  __shared__ __attribute__((aligned(1024))) char smem_[163840];
  LAS char* smem = (LAS char*)smem_;
  const int tid = threadIdx.x, lane = tid & 63;
  const int w = __builtin_amdgcn_readfirstlane(tid >> 6);
  const int xcd = blockIdx.x & 7, idx = blockIdx.x >> 3;
  if (blockIdx.x == 0 && threadIdx.x == 0) *(u64*)(p.ws + WS_STAMP) = __builtin_amdgcn_s_memrealtime();
  if (idx >= 28) { spare_job(p, (idx - 28) * 8 + xcd); STAMP_AT(0, 7); return; }
  const int mt = 4 * (xcd & 1) + idx / 7, cb = 7 * (xcd >> 1) + idx % 7;

  const int ch = lane & 15;
  u32 q_re, q_im, q_dt, base_w, base_me;
  {
    const u16* a0 = p.lre + cb * 16 + ch; const u16* a1 = p.lim + cb * 16 + ch; const u16* a2 = p.ldt + cb * 16 + ch;
    const u32* b0 = (const u32*)(p.ws + WS_BASE) + cb * 32 + w; const u32* b1 = (const u32*)(p.ws + WS_BASE) + cb * 32 + mt;
    asm volatile("global_load_ushort %0, %5, off\n\tglobal_load_ushort %1, %6, off\n\tglobal_load_ushort %2, %7, off\n\t"
                 "global_load_dword %3, %8, off sc1\n\tglobal_load_dword %4, %9, off sc1"
                 : "=&v"(q_re), "=&v"(q_im), "=&v"(q_dt), "=&v"(base_w), "=&v"(base_me)
                 : "v"(a0), "v"(a1), "v"(a2), "v"(b0), "v"(b1) : "memory");
  }

  const int rr = lane >> 3, cp = lane & 7;
  const u32 xrow = (u32)(mt * 256 + w * 32 + 16 * (rr >> 2) + (rr & 3)) * (D * 2);
  const u32 xe = xrow + (u32)((cp ^ (rr >> 1)) * 16), xod = xrow + (u32)((cp ^ (4 + (rr >> 1))) * 16);
  const u32 xo0 = xe, xo1 = xod + (4 * D * 2 - 1024), xo2 = xe + (8 * D * 2 - 2048), xo3 = xod + (12 * D * 2 - 3072);
  const u32 wrow = (u32)(w * 8 + rr) * (F * 2) + cb * 128;
  const u32 wo1 = wrow + (u32)((cp ^ (((rr >> 1) & 1) << 1)) * 16), wo3 = wrow + (u32)((cp ^ ((2 + ((rr >> 1) & 1)) << 1)) * 16);
  const int brow = w * 4 + (rr & 3);
  const u32 bo = (u32)(cb * 16 + (brow & 15)) * (D * 2) + (u32)((cp ^ ((brow >> 1) & 7)) * 16);
  const char* bsrc = (const char*)(w < 4 ? p.bre : p.bim);

  const u32 lds0 = (u32)(uintptr_t)smem;
  auto issue = [&](int kc, int st) {
    const u32 sb = lds0 + st * STAGE;
    const char* xs = (const char*)p.x + kc * 128;
    const char* s1 = (const char*)p.w1 + kc * 64 * F * 2;
    const char* s3 = (const char*)p.w3 + kc * 64 * F * 2;
    const char* sbw = bsrc + kc * 128;
    u32 keep;
    asm volatile(
        "s_mov_b32 %0, m0\n\t"
        "s_mov_b32 m0, %1\n\ts_nop 0\n\t"
        "global_load_lds_dwordx4 %5, %12\n\t"
        "global_load_lds_dwordx4 %6, %12 offset:1024\n\t"
        "global_load_lds_dwordx4 %7, %12 offset:2048\n\t"
        "global_load_lds_dwordx4 %8, %12 offset:3072\n\t"
        "s_mov_b32 m0, %2\n\ts_nop 0\n\t"
        "global_load_lds_dwordx4 %9, %13\n\t"
        "s_mov_b32 m0, %3\n\ts_nop 0\n\t"
        "global_load_lds_dwordx4 %10, %14\n\t"
        "s_mov_b32 m0, %4\n\ts_mov_b32 exec_hi, 0\n\t"
        "global_load_lds_dwordx4 %11, %15\n\t"
        "s_mov_b32 exec_hi, -1\n\t"
        "s_mov_b32 m0, %0"
        : "=&s"(keep)
        : "s"(sb + w * 4096), "s"(sb + OFF_W1 + w * 1024), "s"(sb + OFF_W3 + w * 1024), "s"(sb + OFF_BW + w * 512),
          "v"(xo0), "v"(xo1), "v"(xo2), "v"(xo3), "v"(wo1), "v"(wo3), "v"(bo),
          "s"(xs), "s"(s1), "s"(s3), "s"(sbw)
        : "memory");
  };
  issue(0, 0); issue(1, 1); issue(2, 2);
  SCHED();
  asm volatile("s_waitcnt vmcnt(21)" : "+v"(q_re), "+v"(q_im), "+v"(q_dt), "+v"(base_w), "+v"(base_me) :: "memory");

  cpx lam, coef, lam8, lam32, lam256;
  {
    const float dtf = expf(bf2f(q_dt));
    const float dt = bfhi(pk2(0.f, dtf));
    const float a = bf2f(q_re), b = bf2f(q_im);
    const float m = expf(dt * a);
    float sn, cs; sincosf(dt * b, &sn, &cs);
    lam = {m * cs, m * sn};
    const float nr = lam.r - 1.f, ni = lam.i, inv = 1.f / (a * a + b * b);
    coef = {(nr * a + ni * b) * inv, (ni * a - nr * b) * inv};
    cpx t = cmul(lam, lam); t = cmul(t, t); lam8 = cmul(t, t);
    t = cmul(lam8, lam8); lam32 = cmul(t, t);
    t = cmul(lam32, lam32); t = cmul(t, t); lam256 = cmul(t, t);
  }

  const int r = lane & 31, hk = lane >> 5;
  const u32 rowpart = (u32)(r * 128 + ((hk ^ ((r >> 1) & 7)) * 16));
  const u32 xa = (u32)(w * 4096) + rowpart, ba = OFF_BW + rowpart;
  const int G = lane >> 4, q = (lane & 15) >> 2, pp = lane & 3, mm = pp >> 1;
  const u32 wa = (u32)(OFF_W1 + mm * 8192 + (8 * (G >> 1) + q) * 128 + (2 * mm + ((q >> 1) & 1)) * 32 + 16 * (pp & 1) + 8 * (G & 1));

  auto rd = [&](Frags& f, int st, int s) {
    LAS char* sb = smem + st * STAGE;
    f.x = *(LAS u32x4*)(sb + (xa ^ (u32)(s << 5)));
    f.b = *(LAS u32x4*)(sb + (ba ^ (u32)(s << 5)));
#pragma unroll
    for (int g = 0; g < 4; ++g)
#pragma unroll
      for (int t = 0; t < 2; ++t)
        f.w[g][t] = __builtin_bit_cast(u32x2, __builtin_amdgcn_ds_read_tr16_b64_v4i16((LAS s16x4*)(sb + (wa ^ (u32)(g << 5)) + s * 2048 + t * 512)));
  };
  f32x16 acc[4], accs;
#pragma unroll
  for (int i = 0; i < 16; ++i) { acc[0][i] = 0.f; acc[1][i] = 0.f; acc[2][i] = 0.f; acc[3][i] = 0.f; accs[i] = 0.f; }
  auto mm5 = [&](const Frags& f) {
    accs = mfma(f.x, f.b, accs);
#pragma unroll
    for (int g = 0; g < 4; ++g) {
      u32x4 a = {f.w[g][0][0], f.w[g][0][1], f.w[g][1][0], f.w[g][1][1]};
      acc[g] = mfma(a, f.x, acc[g]);
    }
  };

  Frags fa, fb;
  WAIT_V(14); BAR();
  STAMP_AT(0, 1);
  rd(fa, 0, 0);
#pragma unroll
  for (int c = 0; c < 7; ++c) {
    const int st = c % 3;
    rd(fb, st, 1); SCHED(); mm5(fa); SCHED();
    rd(fa, st, 2); SCHED(); mm5(fb); SCHED();
    rd(fb, st, 3); SCHED(); mm5(fa); SCHED();
    if (c < 6) {
      WAIT_L0();
      if (c < 5) WAIT_V(7); else WAIT_V(0);
      BAR();
      if (c + 3 < 7) issue(c + 3, st);
      rd(fa, (c + 1) % 3, 0);
      SCHED();
    }
    mm5(fb); SCHED();
  }
  STAMP_AT(0, 2);

  cpx u[8];
#pragma unroll
  for (int j = 0; j < 8; ++j) {
    auto sw = __builtin_amdgcn_permlane16_swap(__float_as_uint(accs[j]), __float_as_uint(accs[8 + j]), false, false);
    u[j] = cmul(coef, cpx{__uint_as_float(sw[0]), __uint_as_float(sw[1])});
  }
  cpx e = u[0];
#pragma unroll
  for (int j = 1; j < 8; ++j) e = cfma(lam, e, u[j]);
  cpx eg[4];
#pragma unroll
  for (int g = 0; g < 4; ++g) eg[g] = {__shfl(e.r, g * 16 + ch), __shfl(e.i, g * 16 + ch)};
  {
    cpx agg = cfma(lam8, cfma(lam8, cfma(lam8, eg[0], eg[1]), eg[2]), eg[3]);
    if (lane < 16) *(LAS f32x2*)(smem + OFF_AGG + (w * 16 + ch) * 8) = f32x2{agg.r, agg.i};
  }
  WAIT_L0(); BAR();
  cpx wag[8];
#pragma unroll
  for (int i = 0; i < 8; ++i) { f32x2 v = *(LAS f32x2*)(smem + OFF_AGG + (i * 16 + ch) * 8); wag[i] = {v[0], v[1]}; }
  u64* gran = (u64*)(p.ws + WS_GRAN) + (size_t)cb * 64 * 32;
  if (w > mt) {
    cpx E = wag[0];
#pragma unroll
    for (int i = 1; i < 8; ++i) E = cfma(lam32, E, wag[i]);
    if (lane < 32) {
      const float v = (lane & 16) ? E.i : E.r;
      __hip_atomic_store((GAS u64*)(gran + (w * 8 + mt) * 32 + ch * 2 + (lane >> 4)),
                         ((u64)(base_w + 1u) << 32) | (u64)__float_as_uint(v), RLX_AGENT);
    }
  }
  STAMP_AT(0, 3);

  char* Hb = p.ws + WS_H + (size_t)((mt * 8 + w) * KS2) * 1024 + lane * 16;
#pragma unroll
  for (int g = 0; g < 4; ++g) {
    float hv[8];
#pragma unroll
    for (int j = 0; j < 8; ++j) {
      const float gt = acc[g][(j & 3) + 8 * (j >> 2)], up = acc[g][(j & 3) + 8 * (j >> 2) + 4];
      hv[j] = gt * up * __builtin_amdgcn_rcpf(1.f + __expf(-gt));
    }
    u32x4 o = {pk2(hv[0], hv[1]), pk2(hv[2], hv[3]), pk2(hv[4], hv[5]), pk2(hv[6], hv[7])};
    *(u32x4*)(Hb + (cb * 4 + g) * 1024) = o;
  }
  STAMP_AT(0, 4);

  cpx H = {0.f, 0.f};
  if (mt > 0) {
    const u32 tag = base_me + 1u;
    u64 gr[7], gi[7];
    for (u32 spins = 0; spins < (1u << 16); ++spins) {
      bool ok = true;
#pragma unroll
      for (int i = 0; i < 7; ++i) {
        const int ii = i < mt ? i : 0;
        gr[i] = __hip_atomic_load((GAS u64*)(gran + (mt * 8 + ii) * 32 + ch * 2), RLX_AGENT);
        gi[i] = __hip_atomic_load((GAS u64*)(gran + (mt * 8 + ii) * 32 + ch * 2 + 1), RLX_AGENT);
      }
#pragma unroll
      for (int i = 0; i < 7; ++i) ok = ok && (u32)(gr[i] >> 32) == tag && (u32)(gi[i] >> 32) == tag;
      if (__all(ok)) break;
      __builtin_amdgcn_s_sleep(2);
    }
#pragma unroll
    for (int i = 0; i < 7; ++i)
      if (i < mt) H = cfma(lam256, H, cpx{__uint_as_float((u32)gr[i]), __uint_as_float((u32)gi[i])});
    if (tid == 0) __hip_atomic_store((GAS u32*)(p.ws + WS_BASE) + cb * 32 + mt, tag, RLX_AGENT);
  }
  STAMP_AT(0, 5);
#pragma unroll
  for (int i = 0; i < 7; ++i) if (i < w) H = cfma(lam32, H, wag[i]);
#pragma unroll
  for (int g = 0; g < 3; ++g) { cpx t = cfma(lam8, H, eg[g]); if (g < G) H = t; }
  {
    LAS char* tb = smem + w * 4096;
#pragma unroll
    for (int j = 0; j < 8; ++j) {
      H = cfma(lam, H, u[j]);
      const int rho = (j & 3) + 8 * ((2 * (G & 1) + (j >> 2))) + 4 * (G >> 1);
      *(LAS u32*)(tb + rho * 80 + ch * 4) = pk2(H.r, H.i);
    }
    WAIT_L0();
#pragma unroll
    for (int f = 0; f < 2; ++f) {
      u32x4 o = *(LAS u32x4*)(tb + r * 80 + f * 32 + hk * 16);
      *(u32x4*)(Hb + (112 + cb * 2 + f) * 1024) = o;
    }
  }
  STAMP_AT(0, 6);
}

constexpr int NW2 = 6, NS2 = 28, WIN = 7;
__global__ __launch_bounds__(NW2 * 64, 2) void k2(Params p) {
  __shared__ __attribute__((aligned(16))) char red_[NW2 * 32 * 528];
  LAS char* red = (LAS char*)red_;
  const int tid = threadIdx.x, lane = tid & 63;
  const int w = __builtin_amdgcn_readfirstlane(tid >> 6);
  const int xcd = blockIdx.x & 7, idx = blockIdx.x >> 3;
  const int mt2 = xcd * 4 + idx / 7, nt = idx % 7;
  STAMP_AT(1, 0);
  const u32x4* A0 = (const u32x4*)(p.ws + WS_H + ((size_t)(2 * mt2) * KS2 + w * NS2) * 1024) + lane;
  const u32x4* B0 = (const u32x4*)(p.ws + WS_WT + ((size_t)(2 * nt) * KS2 + w * NS2) * 1024) + lane;
  u32x4 fr[WIN][4];
#pragma unroll
  for (int i = 0; i < WIN; ++i) {
    fr[i][0] = A0[i * 64]; fr[i][1] = B0[i * 64]; fr[i][2] = A0[(KS2 + i) * 64]; fr[i][3] = B0[(KS2 + i) * 64];
  }
  SCHED();
  f32x16 acc[2][2];
#pragma unroll
  for (int i = 0; i < 16; ++i) { acc[0][0][i] = 0.f; acc[0][1][i] = 0.f; acc[1][0][i] = 0.f; acc[1][1][i] = 0.f; }
#pragma unroll
  for (int i = 0; i < NS2; ++i) {
    const u32x4 a0 = fr[i % WIN][0], b0 = fr[i % WIN][1], a1 = fr[i % WIN][2], b1 = fr[i % WIN][3];
    acc[0][0] = mfma(b0, a0, acc[0][0]);
    acc[0][1] = mfma(b0, a1, acc[0][1]);
    acc[1][0] = mfma(b1, a0, acc[1][0]);
    acc[1][1] = mfma(b1, a1, acc[1][1]);
    if (i + WIN < NS2) {
      fr[i % WIN][0] = A0[(i + WIN) * 64]; fr[i % WIN][1] = B0[(i + WIN) * 64];
      fr[i % WIN][2] = A0[(KS2 + i + WIN) * 64]; fr[i % WIN][3] = B0[(KS2 + i + WIN) * 64];
    }
    SCHED();
  }
  STAMP_AT(1, 1);
  {
    const int h = lane >> 5, rho = lane & 31;
#pragma unroll
    for (int ct = 0; ct < 2; ++ct)
#pragma unroll
      for (int tt = 0; tt < 2; ++tt)
#pragma unroll
        for (int q = 0; q < 4; ++q) {
          f32x4 v = {acc[ct][tt][4 * q], acc[ct][tt][4 * q + 1], acc[ct][tt][4 * q + 2], acc[ct][tt][4 * q + 3]};
          *(LAS f32x4*)(red + (w * 32 + tt * 16 + ct * 8 + q * 2 + h) * 528 + rho * 16) = v;
        }
  }
  __syncthreads();
  STAMP_AT(1, 2);
  if (tid < 256) {
    const int cg = tid & 15;
    const u32x2 dv = *(const u32x2*)(p.dsk + nt * 64 + cg * 4);
#pragma unroll
    for (int it = 0; it < 4; ++it) {
      const int rr = (tid >> 4) + it * 16;
      const int tt = rr >> 5, rho = rr & 31;
      const int t = mt2 * 64 + tt * 32 + 16 * ((rho >> 2) & 1) + 4 * (rho >> 3) + (rho & 3);
      const float g = ((const float*)(p.ws + WS_G))[t];
      const u32x2 xv = *(const u32x2*)(p.x + (size_t)t * D + nt * 64 + cg * 4);
      f32x4 sf = {0.f, 0.f, 0.f, 0.f}, ss = {0.f, 0.f, 0.f, 0.f};
#pragma unroll
      for (int ww = 0; ww < NW2; ++ww) {
        const f32x4 v = *(LAS f32x4*)(red + (ww * 32 + tt * 16 + cg) * 528 + rho * 16);
        if (ww < 4) sf += v; else ss += v;
      }
      f32x4 o;
      o[0] = g * (2.f * ss[0] + bflo(xv[0]) * bflo(dv[0])) + (1.f - g) * sf[0];
      o[1] = g * (2.f * ss[1] + bfhi(xv[0]) * bfhi(dv[0])) + (1.f - g) * sf[1];
      o[2] = g * (2.f * ss[2] + bflo(xv[1]) * bflo(dv[1])) + (1.f - g) * sf[2];
      o[3] = g * (2.f * ss[3] + bfhi(xv[1]) * bfhi(dv[1])) + (1.f - g) * sf[3];
      *(f32x4*)(p.out + (size_t)t * D + nt * 64 + cg * 4) = o;
    }
  }
  if (blockIdx.x == 0 && threadIdx.x == 0) { const u64 t0 = *(volatile u64*)(p.ws + WS_STAMP); for (int i = 0; i < 100000 && __builtin_amdgcn_s_memrealtime() - t0 < 2400ull; ++i) __builtin_amdgcn_s_sleep(1); }
}

extern "C" void kernel_launch(void* const* d_in, const int* in_sizes, int n_in, void* d_out, int out_size, void* d_ws,
                              size_t ws_size, hipStream_t stream) {
  Params p;
  p.x = (const u16*)d_in[0]; p.lre = (const u16*)d_in[1]; p.lim = (const u16*)d_in[2];
  p.bre = (const u16*)d_in[3]; p.bim = (const u16*)d_in[4]; p.cre = (const u16*)d_in[5]; p.cim = (const u16*)d_in[6];
  p.ldt = (const u16*)d_in[7]; p.dsk = (const u16*)d_in[8];
  p.w1 = (const u16*)d_in[10]; p.w3 = (const u16*)d_in[11]; p.w2 = (const u16*)d_in[12]; p.gw = (const u16*)d_in[13];
  p.out = (float*)d_out; p.ws = (char*)d_ws;
  k1<<<256, 512, 0, stream>>>(p);
  k2<<<224, NW2 * 64, 0, stream>>>(p);
}
```

```cpp
#include <hip/hip_runtime.h>
#include <stdint.h>

typedef unsigned short u16;
typedef unsigned int u32;
typedef unsigned long long u64;
typedef short s16x4 __attribute__((ext_vector_type(4)));
typedef __bf16 bf16x8 __attribute__((ext_vector_type(8)));
typedef __bf16 bf16x2 __attribute__((ext_vector_type(2)));
typedef float f32x2 __attribute__((ext_vector_type(2)));
typedef float f32x4 __attribute__((ext_vector_type(4)));
typedef float f32x16 __attribute__((ext_vector_type(16)));
typedef u32 u32x2 __attribute__((ext_vector_type(2)));
typedef u32 u32x4 __attribute__((ext_vector_type(4)));
#define LAS __attribute__((address_space(3)))
#define GAS __attribute__((address_space(1)))
#define RLX_AGENT __ATOMIC_RELAXED, __HIP_MEMORY_SCOPE_AGENT

constexpr int D = 448, F = 1792;
constexpr int XROW = D * 2, WROW = F * 2;
constexpr size_t WS_H = 0;
constexpr size_t WS_G = WS_H + (size_t)64 * 28 * 6144;
constexpr size_t WS_GRAN = WS_G + 2048 * 8;
constexpr size_t WS_FLAG = WS_GRAN + 28 * 16 * 256;
constexpr size_t WS_BASE = WS_FLAG + 16 * 1024;
constexpr size_t WS_STAMP = WS_BASE + 4096;
constexpr u32 SPIN_CAP = 1u << 14;
#define AD 4

struct Params {
  const u16 *x, *lre, *lim, *bre, *bim, *cre, *cim, *ldt, *dsk, *w1, *w3, *w2, *gw;
  float* out;
  char* ws;
};

static __device__ __forceinline__ float bf2f(u32 v) { return __uint_as_float(v << 16); }
static __device__ __forceinline__ float bflo(u32 v) { return __uint_as_float(v << 16); }
static __device__ __forceinline__ float bfhi(u32 v) { return __uint_as_float(v & 0xFFFF0000u); }
static __device__ __forceinline__ u32 pk2(float a, float b) {
  f32x2 f = {a, b};
  return __builtin_bit_cast(u32, __builtin_convertvector(f, bf16x2));
}
static __device__ __forceinline__ f32x16 mfma(u32x4 a, u32x4 b, f32x16 c) {
  return __builtin_amdgcn_mfma_f32_32x32x16_bf16(__builtin_bit_cast(bf16x8, a), __builtin_bit_cast(bf16x8, b), c, 0, 0, 0);
}
static __device__ __forceinline__ u32x4 rdtr(LAS char* a) {
  const u32x2 lo = __builtin_bit_cast(u32x2, __builtin_amdgcn_ds_read_tr16_b64_v4i16((LAS s16x4*)a));
  const u32x2 hi = __builtin_bit_cast(u32x2, __builtin_amdgcn_ds_read_tr16_b64_v4i16((LAS s16x4*)(a + 512)));
  return u32x4{lo[0], lo[1], hi[0], hi[1]};
}
static __device__ __forceinline__ void glds1(u32 lds, u32 voff, const void* sb) {
  u32 keep;
  asm volatile("s_mov_b32 %0, m0\n\ts_mov_b32 m0, %1\n\ts_nop 0\n\tglobal_load_lds_dwordx4 %2, %3\n\ts_mov_b32 m0, %0"
               : "=&s"(keep) : "s"(lds), "v"(voff), "s"(sb) : "memory");
}
static __device__ __forceinline__ void glds2(u32 lds, u32 v0, u32 v1, const void* sb) {
  u32 keep;
  asm volatile("s_mov_b32 %0, m0\n\ts_mov_b32 m0, %1\n\ts_nop 0\n\tglobal_load_lds_dwordx4 %2, %4\n\t"
               "global_load_lds_dwordx4 %3, %4 offset:1024\n\ts_mov_b32 m0, %0"
               : "=&s"(keep) : "s"(lds), "v"(v0), "v"(v1), "s"(sb) : "memory");
}
static __device__ __forceinline__ void st16(u32 voff, u32x4 d, const void* sb) {
  asm volatile("global_store_dwordx4 %0, %1, %2 sc1\n\ts_nop 1" :: "v"(voff), "v"(d), "s"(sb) : "memory");
}
template <int I> struct IC { static constexpr int v = I; };
template <int I, int N, class Fn> static __device__ __forceinline__ void sfor(Fn&& f) {
  if constexpr (I < N) { f(IC<I>{}); sfor<I + 1, N>(f); }
}
#define AINL __attribute__((always_inline))
#define WV(n) asm volatile("s_waitcnt vmcnt(%0)" :: "n"(n) : "memory")
#define WAIT_L0() asm volatile("s_waitcnt lgkmcnt(0)" ::: "memory")
#define BAR() __builtin_amdgcn_s_barrier()
#define SCHED() __builtin_amdgcn_sched_barrier(0)

#define STAMP_AT(slot) do {} while (0)
#define STAMP_DECL
#define STAMP_OUT() do {} while (0)

struct cpx { float r, i; };
static __device__ __forceinline__ cpx cmul(cpx a, cpx b) { return {a.r * b.r - a.i * b.i, a.r * b.i + a.i * b.r}; }
static __device__ __forceinline__ cpx cfma(cpx a, cpx b, cpx c) {
  return {fmaf(a.r, b.r, fmaf(-a.i, b.i, c.r)), fmaf(a.r, b.i, fmaf(a.i, b.r, c.i))};
}
static __device__ __forceinline__ cpx csel(bool c, cpx a, cpx b) { return {c ? a.r : b.r, c ? a.i : b.i}; }
static __device__ __forceinline__ float silu(float g) { return g * __builtin_amdgcn_rcpf(1.f + __expf(-g)); }
static __device__ __forceinline__ u32 mk_tag(u32 base) { return base * 0x9E3779B1u + 0x7F4A7C15u; }

constexpr int KB8 = 8192, STG = 40960, O_W1 = 16384, O_W3 = 24576, O_BW = 32768, K19 = 19 * KB8;
static __device__ __forceinline__ constexpr int w1b(int pos) { return (pos < 4 ? 6 + pos : 12 + pos) * KB8; }
static __device__ __forceinline__ constexpr int w3b(int pos) { return (pos < 6 ? 10 + pos : 18) * KB8; }
static __device__ __forceinline__ constexpr int slot(int u) { return (u % 9 < 5 ? u % 9 : (u % 9 == 5 ? 8 : u % 9 - 1)) * 16384; }

static __device__ __forceinline__ void spare_job(const Params& p, int si, int xcd) {
  const int tid = threadIdx.x, lane = tid & 63, w = tid >> 6;
  const int sidx = si * 8 + xcd;
  const u32 base = __hip_atomic_load((GAS u32*)(p.ws + WS_BASE), RLX_AGENT);
  u32x4 xr[8];
  const int lc = lane < 56 ? lane : 55;
#pragma unroll
  for (int i = 0; i < 8; ++i) xr[i] = *(const u32x4*)(p.x + (size_t)(sidx * 64 + w * 8 + i) * D + lc * 8);
  const u32x4 gwv = *(const u32x4*)(p.gw + lc * 8);
  const u32 tag = mk_tag(base);
  float res = 0.f;
#pragma unroll
  for (int i = 0; i < 8; ++i) {
    float s = 0.f;
#pragma unroll
    for (int j = 0; j < 4; ++j) s += bflo(xr[i][j]) * bflo(gwv[j]) + bfhi(xr[i][j]) * bfhi(gwv[j]);
    if (lane >= 56) s = 0.f;
#pragma unroll
    for (int o = 32; o > 0; o >>= 1) s += __shfl_xor(s, o);
    if (lane == i) res = s;
  }
  asm volatile("" :: "v"(tag));
  __syncthreads();
  if (lane < 8)
    __hip_atomic_store((GAS u64*)(p.ws + WS_G) + sidx * 64 + w * 8 + lane,
                       ((u64)tag << 32) | (u64)__float_as_uint(1.f / (1.f + __expf(-res))), RLX_AGENT);
  const int rr = lane >> 3, cp = lane & 7;
  u32x4 acc = {0, 0, 0, 0};
  for (int m = 0; m < 2; ++m) {
    const char* src = (const char*)(m ? p.w3 : p.w1) + (7 * (xcd >> 2)) * 256 + 128 + cp * 16 + (size_t)(si * 112 + rr) * WROW;
    u32x4 v[13];
#pragma unroll
    for (int i = 0; i < 13; ++i) { int it = w + 8 * i; it = it < 98 ? it : 97; v[i] = *(const u32x4*)(src + (it % 7) * 256 + (size_t)(it / 7) * 8 * WROW); }
#pragma unroll
    for (int i = 0; i < 13; ++i) acc ^= v[i];
  }
  {
    u32x4 v[10];
#pragma unroll
    for (int i = 0; i < 10; ++i) {
      int it = sidx * 74 + w + 8 * i; it = it < 2352 ? it : 2351;
      const char* src = it < 1568 ? (const char*)p.w2 + (size_t)it * 1024
                                  : (it < 1960 ? (const char*)p.cre + (size_t)(it - 1568) * 1024 : (const char*)p.cim + (size_t)(it - 1960) * 1024);
      v[i] = *(const u32x4*)(src + lane * 16);
    }
#pragma unroll
    for (int i = 0; i < 10; ++i) acc ^= v[i];
  }
  asm volatile("" :: "v"(acc));
}

struct WF { u32x4 g, u, b; };

__global__ __launch_bounds__(512, 2) void fused(Params p) {
  __shared__ __attribute__((aligned(1024))) char smem_[163840];
  LAS char* smem = (LAS char*)smem_;
  const int tid = threadIdx.x, lane = tid & 63;
  const int w = __builtin_amdgcn_readfirstlane(tid >> 6);
  const int xcd = blockIdx.x & 7, idx = blockIdx.x >> 3;
  STAMP_DECL;
  STAMP_AT(0);
  if (idx >= 28) { spare_job(p, idx - 28, xcd); STAMP_AT(19); STAMP_OUT(); return; }
  const int nt = idx % 7, mt = 4 * (xcd & 3) + idx / 7, cb = 7 * (xcd >> 2) + nt;
  const int rb = w & 3, chh = w >> 2, cbv = 2 * cb + chh;
  const int rot = (cb + 2 * mt) % 7;
  int vm = 0;

  const int ch = lane & 15, G = lane >> 4;
  u32 q_re, q_im, q_dt, basew;
  {
    const u32 o = (u32)(cbv * 16 + ch) * 2;
    asm volatile("global_load_ushort %0, %4, %5\n\tglobal_load_ushort %1, %4, %6\n\tglobal_load_ushort %2, %4, %7\n\t"
                 "global_load_dword %3, %8, %9 sc1"
                 : "=&v"(q_re), "=&v"(q_im), "=&v"(q_dt), "=&v"(basew)
                 : "v"(o), "s"(p.lre), "s"(p.lim), "s"(p.ldt), "v"(0u), "s"(p.ws + WS_BASE) : "memory");
    vm += 4;
  }
  const int mP = vm;

  const int rr = lane >> 3, cp = lane & 7;
  const u32 xrow = (u32)(mt * 128 + (w >> 1) * 32 + 16 * (rr >> 2) + 8 * (w & 1) + (rr & 3)) * XROW;
  const u32 xo0 = xrow + (u32)((cp ^ (rr >> 1)) * 16), xo1 = xrow + (u32)((cp ^ (4 + (rr >> 1))) * 16) + (4 * XROW - 1024);
  const u32 wo = (u32)(w * 8 + rr) * WROW + cb * 256 + (u32)((cp ^ (((rr >> 1) & 1) << 2)) * 16);
  const u32 bo = (u32)(cb * 32 + 16 * (w >> 2) + 8 * (w & 1) + rr) * XROW + (u32)((cp ^ (4 * (w & 1) + (rr >> 1))) * 16);
  const char* bsrc = (const char*)((w & 2) ? p.bim : p.bre);
  const u32 lds0 = (u32)(uintptr_t)smem;
  auto chunk_of = [&](int pos) { const int c = pos + rot; return c >= 7 ? c - 7 : c; };
  auto issue_x = [&](int pos) { glds2(lds0 + (pos % 3) * STG + w * 2048, xo0, xo1, (const char*)p.x + chunk_of(pos) * 128); vm += 2; };
  auto issue_w = [&](int pos) {
    const int c = chunk_of(pos);
    glds1(lds0 + (pos % 3) * STG + O_W1 + w * 1024, wo, (const char*)p.w1 + c * (64 * WROW));
    glds1(lds0 + (pos % 3) * STG + O_W3 + w * 1024, wo, (const char*)p.w3 + c * (64 * WROW));
    vm += 2;
  };
  auto issue_b = [&](int pos) { glds1(lds0 + (pos % 3) * STG + O_BW + w * 1024, bo, bsrc + chunk_of(pos) * 128); vm += 1; };
  auto issue_w1b = [&](int pos) { glds1(lds0 + w1b(pos) + w * 1024, wo, (const char*)p.w1 + chunk_of(pos) * (64 * WROW) + 128); vm += 1; };
  auto issue_w3b = [&](int pos) { glds1(lds0 + w3b(pos) + w * 1024, wo, (const char*)p.w3 + chunk_of(pos) * (64 * WROW) + 128); vm += 1; };
  int mC[7];
  issue_x(0); issue_w(0); issue_b(0); mC[0] = vm;
  BAR();
  issue_x(1); issue_w(1); issue_b(1); mC[1] = vm;
  const u32 fo0 = (u32)(128 * (w >> 2) + 32 * ((w >> 1) & 1) + 16 * (w & 1) + rr) * XROW + nt * 128 + (u32)((cp ^ (((rr >> 1) & 1) << 2)) * 16);
  const u32 crow = (u32)(64 * nt + 16 * (w & 3) + rr) * XROW;
  const char* csrc = (const char*)((w >> 2) ? p.cim : p.cre);
  int mU[21];
  auto issue_u = [&](int u) {
    const int uu = chunk_of(u % 7);
    if (u < 14) glds2(lds0 + slot(u) + w * 2048, fo0, fo0 + (8 * XROW - 1024), (const char*)p.w2 + (256 * uu + 64 * (u / 7)) * XROW);
    else glds2(lds0 + slot(u) + w * 2048, crow + (u32)((cp ^ (rr >> 1)) * 16), crow + (u32)((cp ^ (4 + (rr >> 1))) * 16) + (8 * XROW - 1024), csrc + uu * 128);
    vm += 2; mU[u] = vm;
  };
  if (tid < 512) *(LAS u32*)(smem + K19 + tid * 4) = 0u;
  SCHED();
  asm volatile("s_waitcnt vmcnt(%4)" : "+v"(q_re), "+v"(q_im), "+v"(q_dt), "+v"(basew) : "n"(vm - mP) : "memory");
  STAMP_AT(1);
  const u32 tag = __builtin_amdgcn_readfirstlane(mk_tag(basew));

  cpx lam, coef, lam8, lam32, lam128, m8, mw, p512;
  {
    const float dtf = expf(bf2f(q_dt));
    const float dt = bfhi(pk2(0.f, dtf));
    const float a = bf2f(q_re), b = bf2f(q_im);
    const float m = expf(dt * a);
    float sn, cs; sincosf(dt * b, &sn, &cs);
    lam = {m * cs, m * sn};
    const float nr = lam.r - 1.f, ni = lam.i, inv = 1.f / (a * a + b * b);
    coef = {(nr * a + ni * b) * inv, (ni * a - nr * b) * inv};
    const cpx one = {1.f, 0.f};
    cpx t = cmul(lam, lam); t = cmul(t, t); lam8 = cmul(t, t);
    const cpx l16 = cmul(lam8, lam8); lam32 = cmul(l16, l16);
    const cpx l64 = cmul(lam32, lam32); lam128 = cmul(l64, l64);
    t = cmul(lam128, lam128); const cpx l512 = cmul(t, t), l1024 = cmul(l512, l512);
    m8 = cmul(csel(G & 1, lam8, one), csel(G & 2, l16, one));
    mw = cmul(m8, cmul(csel(rb & 1, lam32, one), csel(rb & 2, l64, one)));
    p512 = cmul(csel(G & 1, one, l512), csel(G & 2, one, l1024));
  }

  const int r = lane & 31, hk = lane >> 5, q = (lane & 15) >> 2, pp = lane & 3;
  const u32 rowpart = (u32)(r * 128 + ((hk ^ ((r >> 1) & 7)) * 16));
  const u32 xa = (u32)(rb * 4096) + rowpart, ba = (u32)(O_BW + chh * 4096) + rowpart;
  const u32 wa = (u32)((8 * hk + q) * 128 + 64 * (chh ^ ((q >> 1) & 1)) + 32 * (G & 1) + 16 * (pp & 1) + 8 * (pp >> 1));

  u32x4 xr[28];
  auto rdx = [&](int pos) {
#pragma unroll
    for (int s = 0; s < 4; ++s) xr[4 * pos + s] = *(LAS u32x4*)(smem + (pos % 3) * STG + (xa ^ (u32)(s << 5)));
  };
  auto rdw = [&](WF& f, int pos, int s) {
    LAS char* sb = smem + (pos % 3) * STG;
    f.g = rdtr(sb + O_W1 + wa + s * 2048);
    f.u = rdtr(sb + O_W3 + wa + s * 2048);
    f.b = *(LAS u32x4*)(sb + (ba ^ (u32)(s << 5)));
  };
  f32x16 ag0, au0, as, ag1, au1;
#pragma unroll
  for (int i = 0; i < 16; ++i) { ag0[i] = 0.f; au0[i] = 0.f; as[i] = 0.f; ag1[i] = 0.f; au1[i] = 0.f; }
  auto mm3 = [&](const WF& f, int i) { ag0 = mfma(f.g, xr[i], ag0); au0 = mfma(f.u, xr[i], au0); as = mfma(xr[i], f.b, as); };
  auto rd2 = [&](int t) {
    return rdtr(smem + (t < 28 ? w1b(t >> 2) : w3b((t - 28) >> 2)) + wa + (t & 3) * 2048);
  };

  STAMP_AT(2);
  WF fa, fb;
  u32x4 wf[3];
  int mW1a = 0, mW1b = 0, mW3a = 0;
  WAIT_L0(); WV(vm - mC[0]); BAR();
  STAMP_AT(3);
  rdx(0); rdw(fa, 0, 0);
  sfor<0, 7>([&](auto c_) AINL {
    constexpr int c = c_.v;
    rdw(fb, c, 1); SCHED(); mm3(fa, 4 * c); SCHED();
    if constexpr (c < 5) issue_x(c + 2); else if constexpr (c == 5) { issue_w1b(0); issue_w1b(1); } else { issue_w3b(0); issue_w3b(1); }
    rdw(fa, c, 2); SCHED(); mm3(fb, 4 * c + 1); SCHED();
    if constexpr (c < 5) issue_w(c + 2); else if constexpr (c == 5) { issue_w1b(2); issue_w1b(3); mW1a = vm; } else { issue_w3b(2); issue_w3b(3); }
    rdw(fb, c, 3); SCHED(); mm3(fa, 4 * c + 2); SCHED();
    if constexpr (c < 5) { issue_b(c + 2); mC[c + 2] = vm; }
    else if constexpr (c == 5) { issue_w1b(4); issue_w1b(5); issue_w1b(6); mW1b = vm; } else { issue_w3b(4); issue_w3b(5); mW3a = vm; }
    WAIT_L0();
    if constexpr (c < 6) WV(vm - mC[c + 1]); else WV(vm - mW1a);
    BAR();
    if constexpr (c == 0) STAMP_AT(4);
    if constexpr (c < 6) { rdx(c + 1); rdw(fa, c + 1, 0); } else { wf[0] = rd2(0); wf[1] = rd2(1); }
    SCHED(); mm3(fb, 4 * c + 3); SCHED();
  });
  STAMP_AT(5);

  const char* Hw = p.ws + WS_H + (size_t)((mt * 4 + rb) * 28 + cbv) * 6144;
  const u32 l16o = (u32)lane * 16;
  const char* flagp = p.ws + WS_FLAG + mt * 1024;
  u32 hp[8];
  cpx uu[8], e, Q, S0, g1, g2, g3;
  f32x2 wg[3];
  u32x4 lb[4];
  u32 pa;
  int mH0 = 0, mW36 = 0, mPA = 0;
  const int kq = w & 3, th = w >> 2;
  const u32 pollo = (u32)(2 * (lane < 7 ? lane : 6) + (kq >> 1)) * 4;
  auto lb_off = [&](int jj) { const int i = mt - 16 + 4 * G + jj; return (u32)((i < 0 ? 0 : i) * 256 + ch * 16); };
  const char* granp = p.ws + WS_GRAN + cbv * 4096;
  auto issue_lb = [&]() {
    asm volatile("global_load_dwordx4 %0, %4, %8 sc1\n\tglobal_load_dwordx4 %1, %5, %8 sc1\n\t"
                 "global_load_dwordx4 %2, %6, %8 sc1\n\tglobal_load_dwordx4 %3, %7, %8 sc1"
                 : "=&v"(lb[0]), "=&v"(lb[1]), "=&v"(lb[2]), "=&v"(lb[3])
                 : "v"(lb_off(0)), "v"(lb_off(1)), "v"(lb_off(2)), "v"(lb_off(3)), "s"(granp) : "memory");
  };
  auto put_flag = [&](int R) {
    if (tid == 0) __hip_atomic_store((GAS u32*)(flagp + R * 256) + cb, tag, RLX_AGENT);
  };
  sfor<0, 56>([&](auto t_) AINL {
    constexpr int t = t_.v;
    if constexpr (t == 14) { WAIT_L0(); WV(vm - mW1b); BAR(); STAMP_AT(6); }
    if constexpr (t == 26) { WAIT_L0(); WV(vm - mH0); BAR(); put_flag(0); STAMP_AT(7); }
    if constexpr (t == 42) { WAIT_L0(); WV(vm - mW36); BAR(); STAMP_AT(8); }
    if constexpr (t + 2 < 56) wf[(t + 2) % 3] = rd2(t + 2);
    if constexpr (t < 28) ag1 = mfma(wf[t % 3], xr[t], ag1); else au1 = mfma(wf[t % 3], xr[t - 28], au1);
    if constexpr (t == 0) issue_u(0);
    if constexpr (t == 2) issue_u(1);
    if constexpr (t == 6) issue_u(2);
    if constexpr (t == 14) issue_u(3);
    if constexpr (t == 16) issue_u(4);
    if constexpr (t == 26) { issue_w3b(6); mW36 = vm; }
    if constexpr (t == 27) issue_u(5);
    if constexpr (t == 42) issue_u(6);
    if constexpr (t == 44) issue_u(7);
    if constexpr (t == 48) { issue_lb(); vm += 4; }
    if constexpr (t == 53) { asm volatile("global_load_dword %0, %1, %2 sc1" : "=v"(pa) : "v"(pollo), "s"(flagp) : "memory"); vm += 1; mPA = vm; }
    if constexpr (t < 8) hp[t] = pk2(silu(ag0[2 * t]) * au0[2 * t], silu(ag0[2 * t + 1]) * au0[2 * t + 1]);
    if constexpr (t == 4) { st16(l16o, u32x4{hp[0], hp[1], hp[2], hp[3]}, Hw); vm += 1; }
    if constexpr (t == 8) { st16(l16o, u32x4{hp[4], hp[5], hp[6], hp[7]}, Hw + 1024); vm += 1; mH0 = vm; }
    if constexpr (t >= 8 && t < 11) {
#pragma unroll
      for (int j = 3 * (t - 8); j < 3 * (t - 8) + 3 && j < 8; ++j) {
        auto sw = __builtin_amdgcn_permlane16_swap(__float_as_uint(as[j]), __float_as_uint(as[8 + j]), false, false);
        uu[j] = cmul(coef, cpx{__uint_as_float(sw[0]), __uint_as_float(sw[1])});
        e = j == 0 ? uu[0] : cfma(lam, e, uu[j]);
      }
    }
    if constexpr (t == 11) {
      g1 = {__shfl(e.r, lane - 16), __shfl(e.i, lane - 16)};
      g2 = {__shfl(e.r, lane - 32), __shfl(e.i, lane - 32)};
      g3 = {__shfl(e.r, lane - 48), __shfl(e.i, lane - 48)};
    }
    if constexpr (t == 13) {
      const cpx z = {0.f, 0.f};
      Q = cfma(lam8, cfma(lam8, csel(G >= 3, g3, z), csel(G >= 2, g2, z)), csel(G >= 1, g1, z));
      e = cfma(lam8, Q, e);
      if (G == 3) *(LAS f32x2*)(smem + K19 + ((chh * 8 + 3 + rb) * 16 + ch) * 8) = f32x2{e.r, e.i};
    }
    if constexpr (t == 14) {
#pragma unroll
      for (int i = 0; i < 3; ++i) wg[i] = *(LAS f32x2*)(smem + K19 + ((chh * 8 + rb + i) * 16 + ch) * 8);
    }
    if constexpr (t == 17) {
      const cpx P = cfma(lam32, cfma(lam32, cpx{wg[0][0], wg[0][1]}, cpx{wg[1][0], wg[1][1]}), cpx{wg[2][0], wg[2][1]});
      S0 = cfma(m8, P, Q);
      const cpx E = cfma(lam32, P, e);
      if (rb == 3 && G == 3)
        st16((u32)(mt * 256 + ch * 16), u32x4{__float_as_uint(E.r), tag, __float_as_uint(E.i), tag}, granp);
    }
    if constexpr (t >= 29 && t < 45) ag1[t - 29] = silu(ag1[t - 29]);
    SCHED();
  });
  STAMP_AT(9);
#pragma unroll
  for (int j = 0; j < 8; ++j) hp[j] = pk2(ag1[2 * j] * au1[2 * j], ag1[2 * j + 1] * au1[2 * j + 1]);
  st16(l16o, u32x4{hp[0], hp[1], hp[2], hp[3]}, Hw + 2048);
  st16(l16o, u32x4{hp[4], hp[5], hp[6], hp[7]}, Hw + 3072);
  vm += 2;

  const int rt = 2 * mt + (xcd >> 2);
  auto check_flag = [&](u32 v, int R) {
    for (u32 spins = 0; !__all(v == tag) && spins < SPIN_CAP; ++spins) {
      __builtin_amdgcn_s_sleep(1);
      asm volatile("global_load_dword %0, %1, %2 sc1\n\ts_waitcnt vmcnt(0)" : "=v"(v) : "v"(pollo), "s"(flagp + R * 256) : "memory");
    }
  };
  asm volatile("s_waitcnt vmcnt(%5)" : "+v"(pa), "+v"(lb[0]), "+v"(lb[1]), "+v"(lb[2]), "+v"(lb[3]) : "n"(vm - mPA) : "memory");
  check_flag(pa, 0);
  STAMP_AT(10);
  const char* Ha = p.ws + WS_H + (size_t)((2 * rt + th) * 28 + kq) * 6144;
  u32x4 af[21][2];
  int mA[21];
  auto issue_a = [&](int u) {
    asm volatile("global_load_dwordx4 %0, %2, %3 sc1\n\tglobal_load_dwordx4 %1, %2, %3 offset:1024 sc1"
                 : "=&v"(af[u][0]), "=&v"(af[u][1]) : "v"(l16o), "s"(Ha + chunk_of(u % 7) * (4 * 6144) + (u / 7) * 2048) : "memory");
    vm += 2; mA[u] = vm;
  };
#pragma unroll
  for (int u = 0; u < AD; ++u) issue_a(u);
  SCHED();

  int mS;
  {
    auto lb_ok = [&]() {
      bool ok = true;
#pragma unroll
      for (int jj = 0; jj < 4; ++jj) ok = ok && (mt - 16 + 4 * G + jj < 0 || (lb[jj][1] == tag && lb[jj][3] == tag));
      return __all(ok);
    };
    for (u32 spins = 0; !lb_ok() && spins < SPIN_CAP; ++spins) {
      __builtin_amdgcn_s_sleep(1);
      issue_lb();
      asm volatile("s_waitcnt vmcnt(0)" : "+v"(lb[0]), "+v"(lb[1]), "+v"(lb[2]), "+v"(lb[3]) :: "memory");
    }
    const cpx z = {0.f, 0.f};
    cpx T = z;
#pragma unroll
    for (int jj = 0; jj < 4; ++jj)
      T = cfma(lam128, T, csel(mt - 16 + 4 * G + jj >= 0, cpx{__uint_as_float(lb[jj][0]), __uint_as_float(lb[jj][2])}, z));
    T = csel(mt - 13 + 4 * G >= 0, cmul(p512, T), z);
    T.r += __shfl_xor(T.r, 16); T.i += __shfl_xor(T.i, 16);
    T.r += __shfl_xor(T.r, 32); T.i += __shfl_xor(T.i, 32);
    cpx H = cfma(mw, T, S0);
    float hr[8], hi[8];
#pragma unroll
    for (int j = 0; j < 8; ++j) { H = cfma(lam, H, uu[j]); hr[j] = H.r; hi[j] = -H.i; }
    LAS char* sc = smem + K19 + w * 1024;
    LAS char* wr = sc + ch * 64 + ((G ^ ((ch >> 1) & 3)) * 16);
    const int c16 = 2 * (pp & 1) + (G & 1);
    LAS char* r0 = sc + (8 * hk + q) * 64 + ((c16 ^ (q >> 1)) * 16) + 8 * (pp >> 1);
    LAS char* r1 = sc + (8 * hk + 4 + q) * 64 + ((c16 ^ (2 + (q >> 1))) * 16) + 8 * (pp >> 1);
#pragma unroll
    for (int f = 0; f < 2; ++f) {
      const float* v = f ? hi : hr;
      *(LAS u32x4*)wr = u32x4{pk2(v[0], v[1]), pk2(v[2], v[3]), pk2(v[4], v[5]), pk2(v[6], v[7])};
      asm volatile("" ::: "memory");
      const u32x2 lo = __builtin_bit_cast(u32x2, __builtin_amdgcn_ds_read_tr16_b64_v4i16((LAS s16x4*)r0));
      const u32x2 hi2 = __builtin_bit_cast(u32x2, __builtin_amdgcn_ds_read_tr16_b64_v4i16((LAS s16x4*)r1));
      asm volatile("" ::: "memory");
      st16(l16o, u32x4{lo[0], lo[1], hi2[0], hi2[1]}, Hw + 4096 + f * 1024);
    }
    vm += 2; mS = vm;
  }
  STAMP_AT(11);

  const u32 fa_l = (u32)((32 * kq + 8 * hk + q) * 128 + 64 * ((q >> 1) & 1) + 32 * (G & 1) + 8 * pp);
  const u32 ca_l = (u32)(r * 128 + (((2 * kq + hk) ^ ((r >> 1) & 7)) * 16));
  u32x4 bf[2][4];
  auto rdb = [&](int u) {
#pragma unroll
    for (int i = 0; i < 4; ++i) {
      if (u < 14) bf[u & 1][i] = rdtr(smem + slot(u) + (fa_l ^ (u32)((i & 1) << 6)) + (i >> 1) * 2048);
      else bf[u & 1][i] = *(LAS u32x4*)(smem + slot(u) + ca_l + (i >> 1) * 8192 + (i & 1) * 4096);
    }
  };
  f32x16 aF[2], aS[2];
#pragma unroll
  for (int i = 0; i < 16; ++i) { aF[0][i] = 0.f; aF[1][i] = 0.f; aS[0][i] = 0.f; aS[1][i] = 0.f; }
  u32 pc, pb, xv[8], dv[2];
  u32x2 gg;
  int mPC = 0, mPB = 0, mE = 0;
  const u32 trow = (u32)(64 * rt + 32 * th + 16 * hk + 4 * kq);
  WAIT_L0(); WV(vm - mA[0]); BAR(); put_flag(1);
  STAMP_AT(12);
  issue_u(8);
  rdb(0);
  sfor<0, 21>([&](auto u_) AINL {
    constexpr int u = u_.v;
    if constexpr (u % 3 == 2 && u < 20) {
      WAIT_L0();
      const int need = u == 2 ? mS : mU[u + 3];
      WV(vm - need); BAR();
      if constexpr (u == 2) put_flag(2);
      if constexpr (u == 2) STAMP_AT(13);
      if constexpr (u == 8) STAMP_AT(14);
      if constexpr (u == 14) STAMP_AT(15);
    }
    if constexpr (u + 1 < 21) rdb(u + 1);
    asm volatile("s_waitcnt vmcnt(%2)" : "+v"(af[u][0]), "+v"(af[u][1]) : "n"(vm - mA[u]) : "memory");
    if constexpr (u < 14) {
      aF[0] = mfma(af[u][0], bf[u & 1][0], aF[0]); aF[1] = mfma(af[u][0], bf[u & 1][1], aF[1]);
      aF[0] = mfma(af[u][1], bf[u & 1][2], aF[0]); aF[1] = mfma(af[u][1], bf[u & 1][3], aF[1]);
    } else {
      aS[0] = mfma(af[u][0], bf[u & 1][0], aS[0]); aS[1] = mfma(af[u][0], bf[u & 1][1], aS[1]);
      aS[0] = mfma(af[u][1], bf[u & 1][2], aS[0]); aS[1] = mfma(af[u][1], bf[u & 1][3], aS[1]);
    }
    SCHED();
    if constexpr (u >= 2 && u + 7 < 21) issue_u(u + 7);
    if constexpr (u + AD == 7) { asm volatile("s_waitcnt vmcnt(%1)" : "+v"(pc) : "n"(vm - mPC) : "memory"); check_flag(pc, 1); }
    if constexpr (u + AD == 14) { asm volatile("s_waitcnt vmcnt(%1)" : "+v"(pb) : "n"(vm - mPB) : "memory"); check_flag(pb, 2); }
    if constexpr (u + AD + 1 == 7) { asm volatile("global_load_dword %0, %1, %2 offset:256 sc1" : "=v"(pc) : "v"(pollo), "s"(flagp) : "memory"); vm += 1; mPC = vm; }
    if constexpr (u + AD + 3 == 14) { asm volatile("global_load_dword %0, %1, %2 offset:512 sc1" : "=v"(pb) : "v"(pollo), "s"(flagp) : "memory"); vm += 1; mPB = vm; }
    if constexpr (u + AD < 21) issue_a(u + AD);
    if constexpr (u == 21 - AD) {
      const u32 xo = trow * XROW + (u32)(64 * nt + r) * 2;
      asm volatile("global_load_dwordx2 %0, %11, %12 sc1\n\t"
                   "global_load_ushort %1, %13, %14\n\tglobal_load_ushort %2, %13, %14 offset:896\n\t"
                   "global_load_ushort %3, %13, %14 offset:1792\n\tglobal_load_ushort %4, %13, %14 offset:2688\n\t"
                   "global_load_ushort %5, %13, %14 offset:64\n\tglobal_load_ushort %6, %13, %14 offset:960\n\t"
                   "global_load_ushort %7, %13, %14 offset:1856\n\tglobal_load_ushort %8, %13, %14 offset:2752\n\t"
                   "global_load_ushort %9, %15, %16\n\tglobal_load_ushort %10, %15, %16 offset:64"
                   : "=&v"(gg), "=&v"(xv[0]), "=&v"(xv[1]), "=&v"(xv[2]), "=&v"(xv[3]), "=&v"(xv[4]), "=&v"(xv[5]), "=&v"(xv[6]), "=&v"(xv[7]),
                     "=&v"(dv[0]), "=&v"(dv[1])
                   : "v"((u32)(64 * rt + 32 * th + r) * 8), "s"(p.ws + WS_G), "v"(xo), "s"(p.x), "v"((u32)(64 * nt + r) * 2), "s"(p.dsk) : "memory");
      vm += 11; mE = vm;
    }
    SCHED();
  });
  STAMP_AT(16);
  asm volatile("s_waitcnt vmcnt(0)" : "+v"(gg), "+v"(xv[0]), "+v"(xv[1]), "+v"(xv[2]), "+v"(xv[3]), "+v"(xv[4]), "+v"(xv[5]), "+v"(xv[6]), "+v"(xv[7]),
               "+v"(dv[0]), "+v"(dv[1]) :: "memory");
  (void)mE; (void)mW3a;
  for (u32 spins = 0; !__all(gg[1] == tag) && spins < SPIN_CAP; ++spins) {
    __builtin_amdgcn_s_sleep(1);
    asm volatile("global_load_dwordx2 %0, %1, %2 sc1\n\ts_waitcnt vmcnt(0)" : "=v"(gg) : "v"((u32)(64 * rt + 32 * th + r) * 8), "s"(p.ws + WS_G) : "memory");
  }
  LAS char* sc = smem + K19 + w * 1024;
  *(LAS u32*)(sc + r * 4) = gg[0];
  asm volatile("" ::: "memory");
  constexpr int RED = 65536;
#pragma unroll
  for (int i = 0; i < 4; ++i) {
    const f32x4 gv = *(LAS f32x4*)(sc + hk * 64 + i * 16);
#pragma unroll
    for (int ct = 0; ct < 2; ++ct) {
      f32x4 o;
#pragma unroll
      for (int j = 0; j < 4; ++j) o[j] = fmaf(gv[j], 2.f * aS[ct][4 * i + j] - aF[ct][4 * i + j], aF[ct][4 * i + j]);
      *(LAS f32x4*)(smem + RED + ((w * 2 + ct) * 4 + i) * 1024 + l16o) = o;
    }
  }
  const f32x4 gm = *(LAS f32x4*)(sc + hk * 64 + kq * 16);
  WAIT_L0(); BAR();
  STAMP_AT(17);
  float* op = p.out + (size_t)trow * D + 64 * nt + r;
#pragma unroll
  for (int ct = 0; ct < 2; ++ct) {
    f32x4 s = *(LAS f32x4*)(smem + RED + (((th * 4) * 2 + ct) * 4 + kq) * 1024 + l16o);
#pragma unroll
    for (int k = 1; k < 4; ++k) s += *(LAS f32x4*)(smem + RED + (((th * 4 + k) * 2 + ct) * 4 + kq) * 1024 + l16o);
#pragma unroll
    for (int j = 0; j < 4; ++j) __hip_atomic_store(op + j * D + ct * 32, fmaf(gm[j], bf2f(xv[ct * 4 + j]) * bf2f(dv[ct]), s[j]), RLX_AGENT);
  }
  if (mt == 15 && cb == 0 && w == 0) {
    for (u32 spins = 0; spins < SPIN_CAP; ++spins) {
      const u64 v = __hip_atomic_load((GAS u64*)(p.ws + WS_G) + r * 64, RLX_AGENT);
      if (__all((u32)(v >> 32) == tag)) break;
      __builtin_amdgcn_s_sleep(1);
    }
    if (lane == 0) __hip_atomic_store((GAS u32*)(p.ws + WS_BASE), tag, RLX_AGENT);
  }
  STAMP_AT(18);
  STAMP_OUT();
}

extern "C" void kernel_launch(void* const* d_in, const int* in_sizes, int n_in, void* d_out, int out_size, void* d_ws,
                              size_t ws_size, hipStream_t stream) {
  Params p;
  p.x = (const u16*)d_in[0]; p.lre = (const u16*)d_in[1]; p.lim = (const u16*)d_in[2];
  p.bre = (const u16*)d_in[3]; p.bim = (const u16*)d_in[4]; p.cre = (const u16*)d_in[5]; p.cim = (const u16*)d_in[6];
  p.ldt = (const u16*)d_in[7]; p.dsk = (const u16*)d_in[8];
  p.w1 = (const u16*)d_in[10]; p.w3 = (const u16*)d_in[11]; p.w2 = (const u16*)d_in[12]; p.gw = (const u16*)d_in[13];
  p.out = (float*)d_out; p.ws = (char*)d_ws;
  fused<<<256, 512, 0, stream>>>(p);
}
```

```cpp
#include <hip/hip_runtime.h>
#include <stdint.h>

typedef unsigned short u16;
typedef unsigned int u32;
typedef unsigned long long u64;
typedef short s16x4 __attribute__((ext_vector_type(4)));
typedef __bf16 bf16x8 __attribute__((ext_vector_type(8)));
typedef __bf16 bf16x2 __attribute__((ext_vector_type(2)));
typedef float f32x2 __attribute__((ext_vector_type(2)));
typedef float f32x4 __attribute__((ext_vector_type(4)));
typedef float f32x16 __attribute__((ext_vector_type(16)));
typedef u32 u32x2 __attribute__((ext_vector_type(2)));
typedef u32 u32x4 __attribute__((ext_vector_type(4)));
#define LAS __attribute__((address_space(3)))
#define GAS __attribute__((address_space(1)))
#define RLX_AGENT __ATOMIC_RELAXED, __HIP_MEMORY_SCOPE_AGENT
#if defined(__HIP_DEVICE_COMPILE__)
#define NOPK __attribute__((target("no-packed-fp32-ops")))
#else
#define NOPK
#endif

constexpr int D = 448, F = 1792;
constexpr int XROW = D * 2, WROW = F * 2;
constexpr size_t WS_H = 0;
constexpr size_t WS_G = WS_H + (size_t)64 * 28 * 6144;
constexpr size_t WS_GRAN = WS_G + 2048 * 8;
constexpr size_t WS_FLAG = WS_GRAN + 28 * 32 * 256;
constexpr size_t WS_BASE = WS_FLAG + 16 * 1024;
constexpr size_t WS_STAMP = WS_BASE + 4096;
constexpr u32 SPIN_CAP = 1u << 14;
#define PD 3
#define ROT1 0
#define ROT2 ((cb + 2 * mt) % 7)
#define CF 4
#define AD 4

struct Params {
  const u16 *x, *lre, *lim, *bre, *bim, *cre, *cim, *ldt, *dsk, *w1, *w3, *w2, *gw;
  float* out;
  char* ws;
};

#define u2f(x) __builtin_bit_cast(float, (u32)(x))
#define f2u(x) __builtin_bit_cast(u32, (float)(x))
static __device__ __forceinline__ float bf2f(u32 v) { return u2f(v << 16); }
static __device__ __forceinline__ float bflo(u32 v) { return u2f(v << 16); }
static __device__ __forceinline__ float bfhi(u32 v) { return u2f(v & 0xFFFF0000u); }
static __device__ __forceinline__ u32 pk2(float a, float b) {
  f32x2 f = {a, b};
  return __builtin_bit_cast(u32, __builtin_convertvector(f, bf16x2));
}
static __device__ __forceinline__ float bperm(int lane4, float v) {
  return u2f((u32)__builtin_amdgcn_ds_bpermute(lane4, (int)f2u(v)));
}
static __device__ __forceinline__ bool all_lanes(bool c) { return __builtin_amdgcn_ballot_w64(!c) == 0; }
#define PIN(x) asm volatile("" : "+v"(x))
#define PINC(c) asm volatile("" : "+v"((c).r), "+v"((c).i))
static __device__ __forceinline__ f32x16 mfma(u32x4 a, u32x4 b, f32x16 c) {
  return __builtin_amdgcn_mfma_f32_32x32x16_bf16(__builtin_bit_cast(bf16x8, a), __builtin_bit_cast(bf16x8, b), c, 0, 0, 0);
}
static __device__ __forceinline__ u32x4 rdtr(LAS char* a) {
  const u32x2 lo = __builtin_bit_cast(u32x2, __builtin_amdgcn_ds_read_tr16_b64_v4i16((LAS s16x4*)a));
  const u32x2 hi = __builtin_bit_cast(u32x2, __builtin_amdgcn_ds_read_tr16_b64_v4i16((LAS s16x4*)(a + 512)));
  return u32x4{lo[0], lo[1], hi[0], hi[1]};
}
template <int LO, int GO> static __device__ __forceinline__ void glds(u32 wb, u32 so, u32 vo, const void* sb) {
  u32 t;
  asm volatile("s_add_u32 m0, %1, %2\n\tv_add_u32 %0, %3, %4\n\tglobal_load_lds_dwordx4 %0, %5 offset:%6"
               : "=&v"(t) : "s"(wb), "n"(LO - GO), "s"(so), "v"(vo), "s"(sb), "n"(GO) : "memory", "scc");
}
template <int O> static __device__ __forceinline__ void st16(u32 voff, u32x4 d, const void* sb) {
  asm volatile("global_store_dwordx4 %0, %1, %2 offset:%3 sc1\n\ts_nop 1" :: "v"(voff), "v"(d), "s"(sb), "n"(O) : "memory");
}
template <int I> struct IC { static constexpr int v = I; };
template <int I, int N, class Fn> static __device__ __forceinline__ void sfor(Fn&& f) {
  if constexpr (I < N) { f(IC<I>{}); sfor<I + 1, N>(f); }
}
#define AINL __attribute__((always_inline))
#define WV(n) asm volatile("s_waitcnt vmcnt(%0)" :: "n"(n) : "memory")
#define WAIT_L0() asm volatile("s_waitcnt lgkmcnt(0)" ::: "memory")
#define BAR() __builtin_amdgcn_s_barrier()
#define SCHED() __builtin_amdgcn_sched_barrier(0)

#define STAMP_AT(slot) do {} while (0)
#define STAMP_DECL
#define STAMP_OUT() do {} while (0)

struct cpx { float r, i; };
static __device__ __forceinline__ cpx cmul(cpx a, cpx b) { return {a.r * b.r - a.i * b.i, a.r * b.i + a.i * b.r}; }
static __device__ __forceinline__ cpx cfma(cpx a, cpx b, cpx c) {
  return {fmaf(a.r, b.r, fmaf(-a.i, b.i, c.r)), fmaf(a.r, b.i, fmaf(a.i, b.r, c.i))};
}
static __device__ __forceinline__ cpx csel(bool c, cpx a, cpx b) { return {c ? a.r : b.r, c ? a.i : b.i}; }
static __device__ __forceinline__ float silu(float g) { return g * __builtin_amdgcn_rcpf(1.f + __expf(-g)); }
static __device__ __forceinline__ u32 mk_tag(u32 base) { return base * 0x9E3779B1u + 0x7F4A7C15u; }

constexpr int KB8 = 8192, K19 = 19 * KB8;
static __device__ __forceinline__ constexpr int xslot(int c) { return (c & 3) * 16384; }
static __device__ __forceinline__ constexpr int bslot(int c) { return ((c & 3) < 2 ? 16 + (c & 3) : 6 + (c & 3)) * KB8; }
static __device__ __forceinline__ constexpr int wslot(int j) { return (10 + (j + 8) % 9) * KB8; }
static __device__ __forceinline__ constexpr int uslot(int u) { return (u % 9) * 16384; }
static __device__ __forceinline__ constexpr int kind(int u) { return u / 7; }

enum { K_LOC = 0, K_BP = 15, K_S1, K_Q1, K_Q2, K_E1, K_RD, K_G0a, K_P1 = K_G0a + 2, K_P2, K_E, K_S0, K_G0b, K_LB = K_G0b + 16, K_G0c,
       K_H0 = K_G0c + 6, K_ST0 = K_H0 + 3, K_H0b, K_ST1 = K_H0b + 2, K_CK0, K_CK1, K_T, K_R1 = K_T + 4, K_R2, K_CH, K_TR = K_CH + 9, K_AD, K_STS = K_AD + 2,
       K_G1, K_END = K_G1 + 24 };
struct Sched { int at[112]; int of[K_END]; };
constexpr bool ring_step(int t) { const int j = (t + PD) / 8, k = (t + PD) % 8; return (k == 0 && j >= 2 && j <= 10) || (k == 2 && j >= 1 && j <= 10); }
constexpr bool a_step(int t) { return t > 110 - 2 * AD && t % 2 == 1 && t < 111; }
constexpr Sched mk_sched() {
  Sched s = {};
  for (int t = 0; t < 112; ++t) s.at[t] = -1;
  int t = 1;
  for (int i = 0; i < K_END; ++i) {
    ++t;
    if (i == K_S1 || i == K_R1 || i == K_R2) ++t;
    if (i == K_RD) { while ((t + PD) % 8 != 0) ++t; }
    else { while (ring_step(t) || a_step(t)) ++t; }
    if (i == K_G0a && t < 30) t = 30;
    s.of[i] = t;
    if (t < 112) s.at[t] = i;
  }
  return s;
}
constexpr Sched SC = mk_sched();
constexpr int bar_after(int t) { while ((t + PD) % 8 != 0) ++t; return t; }
constexpr int g1_done() { int n = 0; for (int i = K_G1; i < K_END; ++i) n += SC.of[i] < 112; return n; }
static_assert(SC.of[K_H0] >= 58 && SC.of[K_G1] >= 86 && SC.of[K_STS] + 8 <= 112 - PD, "schedule");

static __device__ __forceinline__ void spare_job(const Params& p, int si, int xcd) {
  const int tid = threadIdx.x, lane = tid & 63, w = tid >> 6;
  const int sidx = si * 8 + xcd;
  const u32 base = __hip_atomic_load((GAS u32*)(p.ws + WS_BASE), RLX_AGENT);
  u32x4 xr[8];
  const int lc = lane < 56 ? lane : 55;
#pragma unroll
  for (int i = 0; i < 8; ++i) xr[i] = *(const u32x4*)(p.x + (size_t)(sidx * 64 + w * 8 + i) * D + lc * 8);
  const u32x4 gwv = *(const u32x4*)(p.gw + lc * 8);
  const u32 tag = mk_tag(base);
  float res = 0.f;
#pragma unroll
  for (int i = 0; i < 8; ++i) {
    float s = 0.f;
#pragma unroll
    for (int j = 0; j < 4; ++j) s += bflo(xr[i][j]) * bflo(gwv[j]) + bfhi(xr[i][j]) * bfhi(gwv[j]);
    if (lane >= 56) s = 0.f;
#pragma unroll
    for (int o = 32; o > 0; o >>= 1) s += bperm((lane ^ o) * 4, s);
    if (lane == i) res = s;
  }
  asm volatile("" :: "v"(tag));
  BAR();
  if (lane < 8)
    __hip_atomic_store((GAS u64*)(p.ws + WS_G) + sidx * 64 + w * 8 + lane,
                       ((u64)tag << 32) | (u64)f2u(1.f / (1.f + __expf(-res))), RLX_AGENT);
  if (sidx < 28 && tid < 256) st16<0>((u32)(sidx * 8192 + tid * 16), u32x4{0u, tag, 0u, tag}, p.ws + WS_GRAN);
  const int rr = lane >> 3, cp = lane & 7;
  u32x4 acc = {0, 0, 0, 0};
  for (int m = 0; m < 2; ++m) {
    const char* src = (const char*)(m ? p.w3 : p.w1) + (7 * (xcd >> 2)) * 256 + 128 + cp * 16 + (size_t)(si * 112 + rr) * WROW;
    u32x4 v[13];
#pragma unroll
    for (int i = 0; i < 13; ++i) { int it = w + 8 * i; it = it < 98 ? it : 97; v[i] = *(const u32x4*)(src + (it % 7) * 256 + (size_t)(it / 7) * 8 * WROW); }
#pragma unroll
    for (int i = 0; i < 13; ++i) acc ^= v[i];
  }
  {
    u32x4 v[10];
#pragma unroll
    for (int i = 0; i < 10; ++i) {
      int it = sidx * 74 + w + 8 * i; it = it < 2352 ? it : 2351;
      const char* src = it < 1568 ? (const char*)p.w2 + (size_t)it * 1024
                                  : (it < 1960 ? (const char*)p.cre + (size_t)(it - 1568) * 1024 : (const char*)p.cim + (size_t)(it - 1960) * 1024);
      v[i] = *(const u32x4*)(src + lane * 16);
    }
#pragma unroll
    for (int i = 0; i < 10; ++i) acc ^= v[i];
  }
  asm volatile("" :: "v"(acc));
}

template <int LO, int HI> struct LB {
  u32 b[3];
  __device__ __forceinline__ void set(u32 a) {
#pragma unroll
    for (int i = LO; i <= HI; ++i) { b[i] = a + 65536u * i; PIN(b[i]); }
  }
  __device__ __forceinline__ LAS char* at(int off) const { return (LAS char*)(uintptr_t)b[off >> 16] + (off & 65535); }
};

__global__ NOPK __launch_bounds__(512, 2) void fused(Params p) {
  __shared__ __attribute__((aligned(1024))) char smem_[163840];
  LAS char* smem = (LAS char*)smem_;
  const int tid = threadIdx.x, lane = tid & 63;
  const int w = __builtin_amdgcn_readfirstlane(tid >> 6);
  const int xcd = blockIdx.x & 7, idx = blockIdx.x >> 3;
  STAMP_DECL;
  STAMP_AT(0);
  if (idx >= 28) { spare_job(p, idx - 28, xcd); STAMP_AT(19); STAMP_OUT(); return; }
  const int nt = idx % 7, mt = 4 * (xcd & 3) + idx / 7, cb = 7 * (xcd >> 2) + nt;
  const int rb = w & 3, chh = w >> 2, cbv = 2 * cb + chh;
  const int rot = ROT1, rot2 = ROT2;
  int vm = 0;

  const int ch = lane & 15, G = lane >> 4;
  u32 q_re, q_im, q_dt, basew;
  {
    const u32 o = (u32)(cb * 32 + (lane & 31)) * 2;
    asm volatile("global_load_ushort %0, %4, %5\n\tglobal_load_ushort %1, %4, %6\n\tglobal_load_ushort %2, %4, %7\n\t"
                 "global_load_dword %3, %8, %9 sc1"
                 : "=&v"(q_re), "=&v"(q_im), "=&v"(q_dt), "=&v"(basew)
                 : "v"(o), "s"(p.lre), "s"(p.lim), "s"(p.ldt), "v"(0u), "s"(p.ws + WS_BASE) : "memory");
    vm += 4;
  }
  const int mP = vm;

  const int rr = lane >> 3, cp = lane & 7;
  const u32 xrow = (u32)(mt * 128 + (w >> 1) * 32 + 16 * (rr >> 2) + 8 * (w & 1) + (rr & 3)) * XROW;
  const u32 xo0 = xrow + (u32)((cp ^ (rr >> 1)) * 16), xo1 = xrow + (u32)((cp ^ (4 + (rr >> 1))) * 16) + 4 * XROW;
  const u32 bo = (u32)(cb * 32 + 16 * (w >> 2) + 8 * (w & 1) + rr) * XROW + (u32)((cp ^ (4 * (w & 1) + (rr >> 1))) * 16);
  const char* bsrc = (const char*)((w & 2) ? p.bim : p.bre);
  const u32 lds0 = (u32)(uintptr_t)smem;
  const u32 wb1 = lds0 + w * 1024, wb2 = lds0 + w * 2048;
  u32 ck[7], cu[7];
#pragma unroll
  for (int i = 0; i < 7; ++i) { const int c = i + rot, d = i + rot2; ck[i] = c >= 7 ? c - 7 : c; cu[i] = d >= 7 ? d - 7 : d; }
  int mX[7], mW[28], mU[21];
  auto issue_x = [&](auto c_, auto i_) AINL {
    constexpr int c = c_.v, i = i_.v;
    if constexpr (i < 2) glds<xslot(c) + i * 1024, 0>(wb2, ck[c] * 128, i ? xo1 : xo0, p.x);
    else glds<bslot(c), 0>(wb1, ck[c] * 128, bo, bsrc);
    vm += 1; mX[c] = vm;
  };
  sfor<0, 9>([&](auto n_) AINL { issue_x(IC<n_.v / 3>{}, IC<n_.v % 3>{}); if constexpr (n_.v == 2) BAR(); });
  const u32 wo = (u32)(w * 8 + rr) * WROW + cb * 256 + (u32)((cp ^ (((rr >> 1) & 1) << 2)) * 16);
  auto issue_w = [&](auto j_) AINL {
    constexpr int j = j_.v;
    glds<wslot(j), (j / 14) * 128>(wb1, ck[j % 7] * (64 * WROW), wo, (j / 7) & 1 ? p.w3 : p.w1);
    vm += 1; mW[j] = vm;
  };
  const u32 fo0 = (u32)(128 * (w >> 2) + 32 * ((w >> 1) & 1) + 16 * (w & 1) + rr) * XROW + nt * 128 + (u32)((cp ^ (((rr >> 1) & 1) << 2)) * 16);
  const u32 crow = (u32)(64 * nt + 16 * (w & 3) + rr) * XROW;
  const char* csrc = (const char*)((w >> 2) ? p.cim : p.cre);
  auto issue_u = [&](auto u_, auto h_) AINL {
    constexpr int u = u_.v, h = h_.v;
    if constexpr (kind(u) != 1) glds<uslot(u) + h * 1024, 0>(wb2, cu[u % 7] * (256 * XROW) + (kind(u) * 32 + h * 8) * XROW, fo0, p.w2);
    else glds<uslot(u) + h * 1024, 0>(wb2, cu[u % 7] * 128, crow + (u32)((cp ^ (4 * h + (rr >> 1))) * 16) + h * 8 * XROW, csrc);
    vm += 1; mU[u] = vm;
  };
  *(LAS u32*)(smem + K19 + tid * 4) = 0u;
  SCHED();
  asm volatile("s_waitcnt vmcnt(%4)" : "+v"(q_re), "+v"(q_im), "+v"(q_dt), "+v"(basew) : "n"(vm - mP) : "memory");
  STAMP_AT(1);
  const u32 tag = __builtin_amdgcn_readfirstlane(mk_tag(basew));

  constexpr int CT = K19 + 4096;
  if (w == 0) {
    const float dtf = expf(bf2f(q_dt));
    const float dt = bfhi(pk2(0.f, dtf));
    const float a = bf2f(q_re), b = bf2f(q_im);
    const float m = expf(dt * a);
    float sn, cs; sincosf(dt * b, &sn, &cs);
    cpx t = {m * cs, m * sn};
    const float nr = t.r - 1.f, ni = t.i, inv = 1.f / (a * a + b * b);
    LAS f32x2* ct = (LAS f32x2*)(smem + CT) + (lane & 31);
    ct[0] = f32x2{(nr * a + ni * b) * inv, (ni * a - nr * b) * inv};
#pragma unroll
    for (int i = 0; i <= 10; ++i) { ct[32 + 32 * i] = f32x2{t.r, t.i}; t = cmul(t, t); }
  }
  cpx lam, coef, lam8, lam32, lam128, m8, mw, p512;

  const int r = lane & 31, hk = lane >> 5, q = (lane & 15) >> 2, pp = lane & 3;
  const u32 rowpart = (u32)(r * 128 + ((hk ^ ((r >> 1) & 7)) * 16));
  const u32 wa = (u32)((8 * hk + q) * 128 + 64 * (chh ^ ((q >> 1) & 1)) + 32 * (G & 1) + 16 * (pp & 1) + 8 * (pp >> 1));
  LB<1, 2> WA; WA.set(lds0 + wa);
  u32 xs[4];
#pragma unroll
  for (int s = 0; s < 4; ++s) { xs[s] = lds0 + (u32)(rb * 4096) + (rowpart ^ (u32)(s << 5)); PIN(xs[s]); }
  u32 bs[2][4];
#pragma unroll
  for (int s = 0; s < 4; ++s) {
    bs[0][s] = xs[s] + (u32)(chh * 4096 - rb * 4096 + bslot(0)); bs[1][s] = xs[s] + (u32)(chh * 4096 - rb * 4096 + bslot(2));
    PIN(bs[0][s]); PIN(bs[1][s]);
  }
  const f32x16 zero16 = {0.f, 0.f, 0.f, 0.f, 0.f, 0.f, 0.f, 0.f, 0.f, 0.f, 0.f, 0.f, 0.f, 0.f, 0.f, 0.f};
  u32x4 xr[28], wf[PD + 1];
  f32x16 as, acc[4];
  u32 l4, tw, tr, lbo, sw_, sr_;
  auto rdw = [&](int t) AINL { return rdtr(WA.at(wslot(t >> 2) + (t & 3) * 2048)); };

  STAMP_AT(2);
  u32x4 bq[2][4];
  auto xmm = [&](int c, int s) AINL { if (c >= 0) { as = mfma(xr[4 * c + s], bq[c & 1][s], c + s == 0 ? zero16 : as); PIN(as); } SCHED(); };
  sfor<0, 7>([&](auto c_) AINL {
    constexpr int c = c_.v;
    WAIT_L0(); WV(vm - mX[c]); BAR();
    if constexpr (c == 0) STAMP_AT(3);
    if constexpr (c == 3) STAMP_AT(4);
    SCHED();
    auto piece = [&](auto i_) AINL {
      if constexpr (c < 4) issue_x(IC<c + 3>{}, i_); else issue_w(IC<3 * c - 12 + i_.v>{});
      SCHED();
    };
    xmm(c - 1, 0);
    piece(IC<0>{});
#pragma unroll
    for (int s = 0; s < 4; ++s) {
      xr[4 * c + s] = *(LAS u32x4*)((LAS char*)(uintptr_t)xs[s] + xslot(c));
      bq[c & 1][s] = *(LAS u32x4*)((LAS char*)(uintptr_t)bs[(c >> 1) & 1][s] + (c & 1) * KB8);
    }
    SCHED();
    xmm(c - 1, 1); piece(IC<1>{}); xmm(c - 1, 2); piece(IC<2>{}); xmm(c - 1, 3);
    if constexpr (c == 6) {
      WV(vm - mW[1]); BAR();
#pragma unroll
      for (int t = 0; t < PD; ++t) wf[t] = rdw(t);
      SCHED();
#pragma unroll
      for (int s = 0; s < 4; ++s) xmm(6, s);
    }
    if constexpr (c == 2) {
      auto cw = [&](int i) AINL { const f32x2 v = *((LAS f32x2*)(smem + CT) + 32 * i + 16 * chh + ch); return cpx{v[0], v[1]}; };
      const cpx one = {1.f, 0.f};
      coef = cw(0); lam = cw(1); lam8 = cw(4); lam32 = cw(6); lam128 = cw(8);
      m8 = cmul(csel(G & 1, lam8, one), csel(G & 2, cw(5), one));
      mw = cmul(m8, cmul(csel(rb & 1, lam32, one), csel(rb & 2, cw(7), one)));
      p512 = csel(mt - 13 + 4 * G >= 0, cmul(csel(G & 1, one, cw(10)), csel(G & 2, one, cw(11))), cpx{0.f, 0.f});
      PINC(lam); PINC(coef); PINC(lam8); PINC(lam32); PINC(lam128); PINC(m8); PINC(mw); PINC(p512);
    }
    if constexpr (c == 3) {
      l4 = lane * 4;
      tw = lds0 + K19 + (u32)(G == 3 ? ((chh * 8 + 3 + rb) * 16 + ch) * 8 : 2048 + lane * 8);
      tr = lds0 + K19 + (u32)(((chh * 8 + rb) * 16 + ch) * 8);
      lbo = (u32)((mt + 4 * G) * 256 + ch * 16);
      sw_ = lds0 + K19 + (u32)(w * 1024 + ch * 64 + ((G ^ ((ch >> 1) & 3)) * 16));
      sr_ = lds0 + K19 + (u32)(w * 1024 + (8 * hk + q) * 64 + (((2 * (pp & 1) + (G & 1)) ^ (q >> 1)) * 16) + 8 * (pp >> 1));
      PIN(l4); PIN(tw); PIN(tr); PIN(lbo); PIN(sw_); PIN(sr_);
    }
    SCHED();
  });
  STAMP_AT(5);

  const char* Hw = p.ws + WS_H + (size_t)((mt * 4 + rb) * 28 + cbv) * 6144 + 2048;
  const u32 l16o = (u32)lane * 16;
  const char* flagp = p.ws + WS_FLAG + mt * 1024;
  const int kq = w & 3, th = w >> 2, rt = 2 * mt + (xcd >> 2);
  const u32 pollo = (u32)(2 * (lane < 7 ? lane : 6) + (kq >> 1)) * 4;
  const char* granp = p.ws + WS_GRAN + cbv * 8192;
  u32 hp[8], hq[4], pf[3];
  float sg[16], ex[16];
  cpx uu[8], e, Q, P, S0, g1, g2, g3, T;
  f32x2 wg[3];
  u32x4 lb[4], sf[2];
  int mH0 = 0, mS = 0, mH1 = 0, mLB = 0, mF[3];
  auto issue_lb = [&]() AINL {
    asm volatile("global_load_dwordx4 %0, %4, %5 sc1\n\tglobal_load_dwordx4 %1, %4, %5 offset:256 sc1\n\t"
                 "global_load_dwordx4 %2, %4, %5 offset:512 sc1\n\tglobal_load_dwordx4 %3, %4, %5 offset:768 sc1"
                 : "=&v"(lb[0]), "=&v"(lb[1]), "=&v"(lb[2]), "=&v"(lb[3]) : "v"(lbo), "s"(granp) : "memory");
  };
  auto reissue_lb = [&]() AINL {
    asm volatile("global_load_dwordx4 %0, %4, %5 sc1\n\tglobal_load_dwordx4 %1, %4, %5 offset:256 sc1\n\t"
                 "global_load_dwordx4 %2, %4, %5 offset:512 sc1\n\tglobal_load_dwordx4 %3, %4, %5 offset:768 sc1\n\ts_waitcnt vmcnt(0)"
                 : "+v"(lb[0]), "+v"(lb[1]), "+v"(lb[2]), "+v"(lb[3]) : "v"(lbo), "s"(granp) : "memory");
  };
  auto put_flag = [&](auto R_) AINL {
    u32 a, v; const int w_ = w; const u32 tg = tag, fo = (u32)cb * 4; const char* fp = flagp;
    asm volatile("s_cmp_lg_u32 %2, 0\n\ts_cbranch_scc1 1f\n\ts_mov_b64 exec, 1\n\tv_mov_b32 %0, %3\n\tv_mov_b32 %1, %4\n\t"
                 "global_store_dword %0, %1, %5 offset:%6 sc1\n\ts_mov_b64 exec, -1\n1:"
                 : "=&v"(a), "=&v"(v) : "s"(w_), "s"(fo), "s"(tg), "s"(fp), "n"(R_.v * 256) : "memory", "scc");
  };
  auto poll = [&](auto R_) AINL {
    constexpr int R = R_.v;
    asm volatile("global_load_dword %0, %1, %2 offset:%3 sc1" : "=v"(pf[R]) : "v"(pollo), "s"(flagp), "n"(R * 256) : "memory");
    vm += 1; mF[R] = vm;
  };
  auto check = [&](auto R_) AINL {
    constexpr int R = R_.v;
    const u32 po = pollo, tg = tag; const char* fp = flagp; u32 n; u32& v = pf[R]; const int cnt = vm - mF[R];
    asm volatile("s_waitcnt vmcnt(%6)\n\ts_movk_i32 %1, %7\n"
                 "1:\n\tv_cmp_ne_u32 vcc, %4, %0\n\ts_cbranch_vccz 2f\n\ts_sleep 1\n\t"
                 "global_load_dword %0, %2, %3 offset:%5 sc1\n\ts_waitcnt vmcnt(0)\n\ts_sub_u32 %1, %1, 1\n\ts_cbranch_scc0 1b\n2:"
                 : "+v"(v), "=&s"(n) : "v"(po), "s"(fp), "s"(tg), "n"(R * 256), "n"(cnt), "n"(SPIN_CAP) : "memory", "scc", "vcc");
  };
  auto silu_task = [&](const f32x16& g, int n) AINL {
    const int a = 2 * (n / 3), b = a + 1;
    if (n % 3 == 0) {
      const float m = g[a] * -1.44269504f;
      ex[b] = g[b] * -1.44269504f; PIN(ex[b]);
      if (a) { sg[a - 1] = g[a - 1] * ex[a - 1]; PIN(sg[a - 1]); }
      ex[a] = __builtin_amdgcn_exp2f(m); PIN(ex[a]);
    } else if (n % 3 == 1) {
      const float d = 1.f + ex[a];
      ex[b] = __builtin_amdgcn_exp2f(ex[b]); PIN(ex[b]);
      ex[a] = __builtin_amdgcn_rcpf(d); PIN(ex[a]);
    } else {
      const float d = 1.f + ex[b];
      sg[a] = g[a] * ex[a]; PIN(sg[a]);
      ex[b] = __builtin_amdgcn_rcpf(d); PIN(ex[b]);
    }
  };
  auto h_task = [&](const f32x16& g, const f32x16& up, int n) AINL {
#pragma unroll
    for (int o = 5 * n; o < 5 * n + 5; ++o) {
      if (o == 0) { sg[15] = g[15] * ex[15]; PIN(sg[15]); continue; }
      const int gq = (o - 1) / 6, oo = (o - 1) % 6;
      if (oo < 4) { sg[4 * gq + oo] *= up[4 * gq + oo]; PIN(sg[4 * gq + oo]); }
      else { const int i = 2 * gq + oo - 4; hp[i] = pk2(sg[2 * i], sg[2 * i + 1]); PIN(hp[i]); }
    }
  };
  const char* Ha = p.ws + WS_H + (size_t)((2 * rt + th) * 28 + kq) * 6144 + 2048;
  u32x4 af[21][2];
  int mA[21];
  auto issue_a = [&](auto u_) AINL {
    constexpr int u = u_.v, o = kind(u) == 0 ? -2048 : (kind(u) == 1 ? 2048 : 0);
    u32 t;
    asm volatile("v_add_u32 %2, %3, %4\n\tglobal_load_dwordx4 %0, %2, %5 offset:%6 sc1\n\tglobal_load_dwordx4 %1, %2, %5 offset:%7 sc1"
                 : "=&v"(af[u][0]), "=&v"(af[u][1]), "=&v"(t) : "s"(cu[u % 7] * (4 * 6144)), "v"(l16o), "s"(Ha), "n"(o), "n"(o + 1024) : "memory");
    vm += 2; mA[u] = vm;
  };
  const u32 fa_l = (u32)((32 * kq + 8 * hk + q) * 128 + 64 * ((q >> 1) & 1) + 32 * (G & 1) + 8 * pp);
  const u32 ca_l = (u32)(r * 128 + (((2 * kq + hk) ^ ((r >> 1) & 7)) * 16));
  LB<0, 2> FA0, FA1, CA;
  u32 fa_;
  u32x4 bf[2][4];
  auto rdb = [&](int u) AINL {
#pragma unroll
    for (int i = 0; i < 4; ++i) {
      if (kind(u) != 1) bf[u & 1][i] = rdtr(((i & 1) ? FA1 : FA0).at(uslot(u) + (i >> 1) * 2048));
      else bf[u & 1][i] = *(LAS u32x4*)CA.at(uslot(u) + (i >> 1) * 8192 + (i & 1) * 4096);
    }
  };
  const cpx z = {0.f, 0.f};
  u32 bad[4];
  auto lb_bad = [&](int i) AINL { return (lb[i][1] ^ tag) | (lb[i][3] ^ tag); };
  sfor<0, 112>([&](auto t_) AINL {
    constexpr int t = t_.v, j = (t + PD) / 8, k = (t + PD) % 8, K = SC.at[t];
    constexpr int TA = bar_after(SC.of[K_ST1] + 8);
    if constexpr (k == 0) {
      if constexpr (K == K_RD) asm volatile("s_waitcnt lgkmcnt(%0)" :: "n"(2 * (t - SC.of[K_E1] - 1)) : "memory");
      int need = j < 14 ? mW[j < 14 ? 2 * j + 1 : 0] : mS;
      if constexpr (t == TA) need = need > mH0 ? need : mH0;
      WV(vm - need); BAR();
      if constexpr (t == TA) put_flag(IC<0>{});
      if constexpr (j == 14) put_flag(IC<1>{});
    }
    if constexpr (t == 28) STAMP_AT(6);
    if constexpr (t == 56) STAMP_AT(7);
    if constexpr (t == 84) STAMP_AT(8);
    SCHED();
    acc[t / 28] = mfma(wf[t % (PD + 1)], xr[t % 28], t % 28 == 0 ? zero16 : acc[t / 28]); PIN(acc[t / 28]);
    SCHED();
    if constexpr (t + PD < 112) wf[(t + PD) % (PD + 1)] = rdw(t + PD);
    SCHED();
    if constexpr (K >= K_LOC && K < K_BP) {
      if constexpr (K == 0 || K % 2 == 1) {
        constexpr int i = (K + 1) / 2;
        auto sw = __builtin_amdgcn_permlane16_swap(f2u(as[i]), f2u(as[8 + i]), false, false);
        uu[i] = cmul(coef, cpx{u2f(sw[0]), u2f(sw[1])}); PINC(uu[i]);
      } else { e = cfma(lam, K == 2 ? uu[0] : e, uu[K / 2]); PINC(e); }
    }
    if constexpr (K == K_BP) {
      g1 = {bperm(l4 + 192, e.r), bperm(l4 + 192, e.i)};
      g2 = {bperm(l4 + 128, e.r), bperm(l4 + 128, e.i)};
      g3 = {bperm(l4 + 64, e.r), bperm(l4 + 64, e.i)};
    }
    if constexpr (K == K_S1) { g3 = csel(G >= 3, g3, z); g2 = csel(G >= 2, g2, z); g1 = csel(G >= 1, g1, z); PINC(g1); PINC(g2); PINC(g3); }
    if constexpr (K == K_Q1) { Q = cfma(lam8, g3, g2); PINC(Q); }
    if constexpr (K == K_Q2) { Q = cfma(lam8, Q, g1); PINC(Q); }
    if constexpr (K == K_E1) {
      e = cfma(lam8, Q, e);
      *(LAS f32x2*)(uintptr_t)tw = f32x2{e.r, e.i};
    }
    if constexpr (K == K_RD) {
#pragma unroll
      for (int i = 0; i < 3; ++i) wg[i] = *((LAS f32x2*)(uintptr_t)tr + 16 * i);
    }
    if constexpr (K == K_P1) { P = cfma(lam32, cpx{wg[0][0], wg[0][1]}, cpx{wg[1][0], wg[1][1]}); PINC(P); }
    if constexpr (K == K_P2) { P = cfma(lam32, P, cpx{wg[2][0], wg[2][1]}); PINC(P); }
    if constexpr (K == K_E) {
      const cpx E = cfma(lam32, P, e);
      const int rb_ = rb; const u32 o = lbo; const char* gp = granp;
      asm volatile("s_cmp_lg_u32 %0, 3\n\ts_cbranch_scc1 1f\n\ts_mov_b32 exec_lo, 0\n\ts_mov_b32 exec_hi, 0xffff0000\n\t"
                   "global_store_dwordx4 %1, %2, %3 offset:1024 sc1\n\ts_mov_b64 exec, -1\n\ts_nop 0\n1:"
                   :: "s"(rb_), "v"(o), "v"(u32x4{f2u(E.r), tag, f2u(E.i), tag}), "s"(gp) : "memory", "scc");
    }
    if constexpr (K == K_S0) { S0 = cfma(m8, P, Q); PINC(S0); }
    if constexpr (K >= K_G0a && K < K_P1) silu_task(acc[0], K - K_G0a);
    if constexpr (K >= K_G0b && K < K_LB) silu_task(acc[0], K - K_G0b + 2);
    if constexpr (K >= K_G0c && K < K_H0) silu_task(acc[0], K - K_G0c + 18);
    if constexpr (K >= K_H0 && K < K_ST0) h_task(acc[0], acc[1], K - K_H0);
    if constexpr (K >= K_H0b && K < K_ST1) h_task(acc[0], acc[1], K - K_H0b + 3);
    if constexpr (K == K_ST0) { st16<-2048>(l16o, u32x4{hp[0], hp[1], hp[2], hp[3]}, Hw); vm += 1; }
    if constexpr (K == K_ST1) { st16<-1024>(l16o, u32x4{hp[4], hp[5], hp[6], hp[7]}, Hw); vm += 1; mH0 = vm; }
    if constexpr (K >= K_G1) silu_task(acc[2], K - K_G1);
    if constexpr (K == K_LB) { issue_lb(); vm += 4; mLB = vm; }
    if constexpr (K == K_CK0) {
      asm volatile("s_waitcnt vmcnt(%4)" : "+v"(lb[0]), "+v"(lb[1]), "+v"(lb[2]), "+v"(lb[3]) : "n"(vm - mLB) : "memory");
#pragma unroll
      for (int i = 0; i < 4; ++i) { bad[i] = lb_bad(i); PIN(bad[i]); }
    }
    if constexpr (K == K_CK1) {
      const u32 bd_ = bad[0] | bad[1] | bad[2] | bad[3];
      if (__builtin_expect(!all_lanes(bd_ == 0), 0)) {
        u32 spins = 0;
        do {
          __builtin_amdgcn_s_sleep(1);
          reissue_lb();
        } while (!all_lanes((lb_bad(0) | lb_bad(1) | lb_bad(2) | lb_bad(3)) == 0) && ++spins < SPIN_CAP);
      }
    }
    if constexpr (K >= K_T && K < K_T + 3) {
      T = cfma(lam128, K == K_T ? cpx{u2f(lb[0][0]), u2f(lb[0][2])} : T, cpx{u2f(lb[K - K_T + 1][0]), u2f(lb[K - K_T + 1][2])}); PINC(T);
    }
    if constexpr (K == K_T + 3) { T = cmul(p512, T); g1 = {bperm(l4 + 128, T.r), bperm(l4 + 128, T.i)}; }
    if constexpr (K == K_R1) { T.r += g1.r; T.i += g1.i; g1 = {bperm(l4 + 64, T.r), bperm(l4 + 64, T.i)}; }
    if constexpr (K == K_R2) { T.r += g1.r; T.i += g1.i; T = cfma(mw, T, S0); PINC(T); }
    if constexpr (K >= K_CH && K < K_TR) {
      constexpr int i = K - K_CH;
      if constexpr (i < 8) { T = cfma(lam, T, uu[i]); PINC(T); uu[i] = T; }
      if constexpr (i >= 2 && i % 2 == 0) { hp[i / 2 - 1] = pk2(uu[i - 2].r, uu[i - 1].r); PIN(hp[i / 2 - 1]); }
      if constexpr (i >= 3 && i % 2 == 1) { hq[i / 2 - 1] = pk2(-uu[i - 3].i, -uu[i - 2].i); PIN(hq[i / 2 - 1]); }
      if constexpr (i == 8) { hq[3] = pk2(-uu[6].i, -uu[7].i); PIN(hq[3]); tr = sr_ ^ 32u; PIN(tr); }
    }
    if constexpr (K == K_TR) {
#pragma unroll
      for (int f = 0; f < 2; ++f) {
        *(LAS u32x4*)(uintptr_t)sw_ = f ? u32x4{hq[0], hq[1], hq[2], hq[3]} : u32x4{hp[0], hp[1], hp[2], hp[3]};
        asm volatile("" ::: "memory");
        const u32x2 lo = __builtin_bit_cast(u32x2, __builtin_amdgcn_ds_read_tr16_b64_v4i16((LAS s16x4*)(uintptr_t)sr_));
        const u32x2 hi = __builtin_bit_cast(u32x2, __builtin_amdgcn_ds_read_tr16_b64_v4i16((LAS s16x4*)((LAS char*)(uintptr_t)tr + 256)));
        asm volatile("" ::: "memory");
        sf[f] = u32x4{lo[0], lo[1], hi[0], hi[1]};
      }
    }
    if constexpr (K == K_AD) { fa_ = lds0 + fa_l; PIN(fa_); }
    if constexpr (K == K_AD + 1) { FA0.set(fa_); FA1.set(fa_ ^ 64u); }
    if constexpr (K == K_STS) { st16<2048>(l16o, sf[0], Hw); st16<3072>(l16o, sf[1], Hw); vm += 2; mS = vm; }
    if constexpr (t == 110) rdb(0);
    SCHED();
    if constexpr (k == 0 && j >= 2 && j <= 10) issue_w(IC<2 * j + 6>{});
    if constexpr (k == 2 && j >= 1 && j <= 10) issue_w(IC<2 * j + 7>{});
    if constexpr ((k == 4 || k == 6) && j <= 8 && j % 2 == 0) issue_u(IC<j / 2>{}, IC<(k - 4) / 2>{});
    if constexpr ((k == 0 || k == 1) && j >= 11) issue_u(IC<j - 6>{}, IC<k>{});
    if constexpr (t == TA + 7) poll(IC<0>{});
    if constexpr (t == 110 - 2 * AD) check(IC<0>{});

    if constexpr (a_step(t)) issue_a(IC<(t - 110 + 2 * AD) / 2>{});
    SCHED();
  });
  STAMP_AT(9);

  f32x16 aF[2], aS[2];
  u32x2 gg, xv[2], dv;
  float sk[8], gl = 0.f;
  u32 ra[8];
  int mG = 0, mE = 0;
  const u32 go = (u32)(64 * rt + 32 * th + 16 * ((r >> 2) & 1) + 4 * (r >> 3) + (r & 3)) * 8;
  const u32 orow = (u32)(64 * rt + 32 * th + 4 * kq + G), ocol = (u32)(64 * nt + 4 * ch);
  constexpr int RED = 49152;
  sfor<0, 21>([&](auto u_) AINL {
    constexpr int u = u_.v;
    if constexpr (u % 3 == 1 && u <= 16) {
      if constexpr (u > 1) {
        int need = mU[u == 16 ? 20 : u + 3];
        if constexpr (u == CF) need = need > mH1 ? need : mH1;
        WV(vm - need);
      }
      BAR();
      if constexpr (u == CF) put_flag(IC<2>{});
      if constexpr (u == 1) STAMP_AT(10);
      if constexpr (u == 4) STAMP_AT(11);
      if constexpr (u == 7) STAMP_AT(12);
      if constexpr (u == 10) STAMP_AT(13);
      if constexpr (u == 13) STAMP_AT(14);
      if constexpr (u == 16) STAMP_AT(15);
    }
    if constexpr (u + 1 < 21) rdb(u + 1);
    asm volatile("s_waitcnt vmcnt(%2)" : "+v"(af[u][0]), "+v"(af[u][1]) : "n"(vm - mA[u]) : "memory");
    f32x16* a = kind(u) == 1 ? aS : aF;
    constexpr bool first = u == 0 || (u == 7 && kind(0) + kind(7) == 1);
    auto fill = [&](int n) AINL {
      SCHED();
#pragma unroll
      for (int x = g1_done() + 8 * u + 2 * n; x < g1_done() + 8 * u + 2 * n + 2; ++x) {
        if (x < 24) silu_task(acc[2], x);
        else if (x < 29) h_task(acc[2], acc[3], x - 24);
        else if (x == 29) {
          st16<0>(l16o, u32x4{hp[0], hp[1], hp[2], hp[3]}, Hw);
          st16<1024>(l16o, u32x4{hp[4], hp[5], hp[6], hp[7]}, Hw);
          vm += 2; mH1 = vm;
        }
      }
      SCHED();
    };
    a[0] = mfma(bf[u & 1][0], af[u][0], first ? zero16 : a[0]); if constexpr (u < 4) fill(0);
    a[1] = mfma(bf[u & 1][1], af[u][0], first ? zero16 : a[1]); if constexpr (u < 4) fill(1);
    a[0] = mfma(bf[u & 1][2], af[u][1], a[0]); if constexpr (u < 4) fill(2);
    a[1] = mfma(bf[u & 1][3], af[u][1], a[1]); PIN(a[0]); PIN(a[1]); if constexpr (u < 4) fill(3);
    SCHED();
    if constexpr (u == 4) CA.set(lds0 + ca_l);
    if constexpr (u == 1) { issue_u(IC<9>{}, IC<0>{}); issue_u(IC<9>{}, IC<1>{}); }
    if constexpr (u >= 4 && u + 6 < 21) { issue_u(IC<u + 6>{}, IC<0>{}); issue_u(IC<u + 6>{}, IC<1>{}); }
    if constexpr (u + AD == 7) check(IC<1>{});
    if constexpr (u + AD == 14) check(IC<2>{});
    if constexpr (u + AD + 2 == 7) poll(IC<1>{});
    if constexpr (u + AD + 2 == 14) poll(IC<2>{});
    if constexpr (u + AD < 21) issue_a(IC<u + AD>{});
    if constexpr (u == 3) {
      asm volatile("global_load_dwordx2 %0, %4, %5 sc1\n\tglobal_load_dwordx2 %1, %6, %8\n\tglobal_load_dwordx2 %2, %7, %8\n\t"
                   "global_load_dwordx2 %3, %9, %10"
                   : "=&v"(gg), "=&v"(xv[0]), "=&v"(xv[1]), "=&v"(dv)
                   : "v"(go), "s"(p.ws + WS_G), "v"(orow * XROW + ocol * 2), "v"((orow + 16) * XROW + ocol * 2), "s"(p.x), "v"(ocol * 2), "s"(p.dsk) : "memory");
      vm += 4; mE = vm;
    }
    if constexpr (u == 9) {
      asm volatile("s_waitcnt vmcnt(%4)" : "+v"(gg), "+v"(xv[0]), "+v"(xv[1]), "+v"(dv) : "n"(vm - mE) : "memory");
      if (__builtin_expect(!all_lanes(gg[1] == tag), 0)) {
        u32 spins = 0;
        do {
          __builtin_amdgcn_s_sleep(1);
          asm volatile("global_load_dwordx2 %0, %1, %2 sc1\n\ts_waitcnt vmcnt(0)" : "+v"(gg) : "v"(go), "s"(p.ws + WS_G) : "memory");
        } while (!all_lanes(gg[1] == tag) && ++spins < SPIN_CAP);
      }
      gl = u2f(gg[0]);
#pragma unroll
      for (int s = 0; s < 2; ++s) {
        const float gs = bperm((8 * kq + 4 * s + G) * 4, gl);
        sk[4 * s] = gs * (bflo(xv[s][0]) * bflo(dv[0])); sk[4 * s + 1] = gs * (bfhi(xv[s][0]) * bfhi(dv[0]));
        sk[4 * s + 2] = gs * (bflo(xv[s][1]) * bflo(dv[1])); sk[4 * s + 3] = gs * (bfhi(xv[s][1]) * bfhi(dv[1]));
      }
#pragma unroll
      for (int i = 0; i < 8; ++i) PIN(sk[i]);
    }
    if constexpr (u == 15 || u == 16) {
      const float g2x = gl + gl;
#pragma unroll
      for (int i = 0; i < 16; ++i) { aS[u - 15][i] *= g2x; }
      PIN(aS[u - 15]);
    }
    if constexpr (u == 17) {
#pragma unroll
      for (int i = 0; i < 8; ++i) { ra[i] = lds0 + (u32)(RED + w * 8192 + r * 256 + (((2 * i + hk) ^ (r & 15)) * 16)); PIN(ra[i]); }
      gl = 1.f - gl; PIN(gl);
    }
    SCHED();
  });
  STAMP_AT(16);
  (void)mG;
#pragma unroll
  for (int i = 0; i < 8; ++i) {
    f32x4 o;
#pragma unroll
    for (int jj = 0; jj < 4; ++jj) o[jj] = fmaf(gl, aF[i >> 2][4 * (i & 3) + jj], aS[i >> 2][4 * (i & 3) + jj]);
    *(LAS f32x4*)(uintptr_t)ra[i] = o;
  }
  WAIT_L0(); BAR();
  STAMP_AT(17);
#pragma unroll
  for (int s = 0; s < 2; ++s) {
    const int rw = 8 * kq + 4 * s + G;
    LAS char* a = smem + RED + th * 32768 + rw * 256 + ((ch ^ (rw & 15)) * 16);
    f32x4 v = *(LAS f32x4*)a;
    v = (v + *(LAS f32x4*)(a + 8192)) + (*(LAS f32x4*)(a + 16384) + *(LAS f32x4*)(a + 24576));
    st16<0>((orow + 16 * s) * (D * 4) + ocol * 4,
            u32x4{f2u(v[0] + sk[4 * s]), f2u(v[1] + sk[4 * s + 1]), f2u(v[2] + sk[4 * s + 2]), f2u(v[3] + sk[4 * s + 3])}, p.out);
  }
  if (mt == 15 && cb == 0 && w == 0) {
    for (u32 spins = 0; spins < SPIN_CAP; ++spins) {
      const u64 v = __hip_atomic_load((GAS u64*)(p.ws + WS_G) + r * 64, RLX_AGENT);
      if (all_lanes((u32)(v >> 32) == tag)) break;
      __builtin_amdgcn_s_sleep(1);
    }
    if (lane == 0) __hip_atomic_store((GAS u32*)(p.ws + WS_BASE), tag, RLX_AGENT);
  }
  STAMP_AT(18);
  STAMP_OUT();
}

extern "C" void kernel_launch(void* const* d_in, const int* in_sizes, int n_in, void* d_out, int out_size, void* d_ws,
                              size_t ws_size, hipStream_t stream) {
  Params p;
  p.x = (const u16*)d_in[0]; p.lre = (const u16*)d_in[1]; p.lim = (const u16*)d_in[2];
  p.bre = (const u16*)d_in[3]; p.bim = (const u16*)d_in[4]; p.cre = (const u16*)d_in[5]; p.cim = (const u16*)d_in[6];
  p.ldt = (const u16*)d_in[7]; p.dsk = (const u16*)d_in[8];
  p.w1 = (const u16*)d_in[10]; p.w3 = (const u16*)d_in[11]; p.w2 = (const u16*)d_in[12]; p.gw = (const u16*)d_in[13];
  p.out = (float*)d_out; p.ws = (char*)d_ws;
  fused<<<256, 512, 0, stream>>>(p);
}
```

```cpp
#include <hip/hip_runtime.h>
#include <stdint.h>

typedef unsigned short u16;
typedef unsigned int u32;
typedef unsigned long long u64;
typedef short s16x4 __attribute__((ext_vector_type(4)));
typedef __bf16 bf16x8 __attribute__((ext_vector_type(8)));
typedef __bf16 bf16x2 __attribute__((ext_vector_type(2)));
typedef float f32x2 __attribute__((ext_vector_type(2)));
typedef float f32x4 __attribute__((ext_vector_type(4)));
typedef float f32x16 __attribute__((ext_vector_type(16)));
typedef u32 u32x2 __attribute__((ext_vector_type(2)));
typedef u32 u32x4 __attribute__((ext_vector_type(4)));
#define LAS __attribute__((address_space(3)))
#define GAS __attribute__((address_space(1)))
#define RLX_AGENT __ATOMIC_RELAXED, __HIP_MEMORY_SCOPE_AGENT
#if defined(__HIP_DEVICE_COMPILE__)
#define NOPK __attribute__((target("no-packed-fp32-ops")))
#else
#define NOPK
#endif

constexpr int D = 448, F = 1792;
constexpr int XROW = D * 2, WROW = F * 2;
constexpr size_t WS_H = 0;
constexpr size_t WS_G = WS_H + (size_t)64 * 28 * 6144;
constexpr size_t WS_GRAN = WS_G + 2048 * 8;
constexpr size_t WS_FLAG = WS_GRAN + 28 * 32 * 256;
#define FSEP 0
constexpr int FR = FSEP ? 128 : 256, FC = FSEP ? 512 : 4, FM = FSEP ? 8192 : 1024;
constexpr size_t WS_BASE = WS_FLAG + 16 * FM;
constexpr size_t WS_STAMP = WS_BASE + 4096;
constexpr u32 SPIN_CAP = 1u << 14;
#define PD 3
#define ROT1 0
#define ROT2 ((cb + 2 * mt) % 7)
#define TC0 99
#define TC1 119
#define TC2 151
#define PL 9
#define TD 106
#define FD0 10
#define FD1 12
#define FD2 12
#define TG 164
#define RT0 0
#define RT1 100
#define LBD 15
#define NG0A 1
#define LEAD 14
#define TEP 90

struct Params {
  const u16 *x, *lre, *lim, *bre, *bim, *cre, *cim, *ldt, *dsk, *w1, *w3, *w2, *gw;
  float* out;
  char* ws;
};

#define u2f(x) __builtin_bit_cast(float, (u32)(x))
#define f2u(x) __builtin_bit_cast(u32, (float)(x))
static __device__ __forceinline__ float bf2f(u32 v) { return u2f(v << 16); }
static __device__ __forceinline__ float bflo(u32 v) { return u2f(v << 16); }
static __device__ __forceinline__ float bfhi(u32 v) { return u2f(v & 0xFFFF0000u); }
static __device__ __forceinline__ u32 pk2(float a, float b) {
  f32x2 f = {a, b};
  return __builtin_bit_cast(u32, __builtin_convertvector(f, bf16x2));
}
static __device__ __forceinline__ float bperm(int lane4, float v) {
  return u2f((u32)__builtin_amdgcn_ds_bpermute(lane4, (int)f2u(v)));
}
static __device__ __forceinline__ void swap16(float& a, float& b) {
  auto r = __builtin_amdgcn_permlane16_swap(f2u(a), f2u(b), false, false); a = u2f(r[0]); b = u2f(r[1]);
}
static __device__ __forceinline__ void swap32(float& a, float& b) {
  auto r = __builtin_amdgcn_permlane32_swap(f2u(a), f2u(b), false, false); a = u2f(r[0]); b = u2f(r[1]);
}
static __device__ __forceinline__ bool all_lanes(bool c) { return __builtin_amdgcn_ballot_w64(!c) == 0; }
#define PIN(x) asm volatile("" : "+v"(x))
#define PINS(x) asm volatile("" : "+s"(x))
#define PINC(c) asm volatile("" : "+v"((c).r), "+v"((c).i))
static __device__ __forceinline__ f32x16 mfma(u32x4 a, u32x4 b, f32x16 c) {
  return __builtin_amdgcn_mfma_f32_32x32x16_bf16(__builtin_bit_cast(bf16x8, a), __builtin_bit_cast(bf16x8, b), c, 0, 0, 0);
}
static __device__ __forceinline__ u32x4 rdtr(LAS char* a) {
  const u32x2 lo = __builtin_bit_cast(u32x2, __builtin_amdgcn_ds_read_tr16_b64_v4i16((LAS s16x4*)a));
  const u32x2 hi = __builtin_bit_cast(u32x2, __builtin_amdgcn_ds_read_tr16_b64_v4i16((LAS s16x4*)(a + 512)));
  return u32x4{lo[0], lo[1], hi[0], hi[1]};
}
template <int LO, int GO> static __device__ __forceinline__ void glds(u32 wb, u32 so, u32 vo, const void* sb) {
  asm volatile("s_add_u32 m0, %0, %1\n\ts_nop 0\n\tglobal_load_lds_dwordx4 %2, %3 offset:%4"
               :: "s"(wb), "n"(LO - GO), "v"(vo), "s"((const char*)sb + so), "n"(GO) : "memory", "scc");
}
template <int O> static __device__ __forceinline__ void st16(u32 voff, u32x4 d, const void* sb) {
  asm volatile("global_store_dwordx4 %0, %1, %2 offset:%3 sc1\n\ts_nop 1" :: "v"(voff), "v"(d), "s"(sb), "n"(O) : "memory");
}
template <int O> static __device__ __forceinline__ void sth(u32 voff, u32x4 d, const void* sb) {
  st16<O>(voff, d, sb);
}
constexpr int SV = 1;
template <int I> struct IC { static constexpr int v = I; };
template <int I, int N, class Fn> static __device__ __forceinline__ void sfor(Fn&& f) {
  if constexpr (I < N) { f(IC<I>{}); sfor<I + 1, N>(f); }
}
#define AINL __attribute__((always_inline))
#define WV(n) asm volatile("s_waitcnt vmcnt(%0)" :: "n"(n) : "memory")
#define WAIT_L0() asm volatile("s_waitcnt lgkmcnt(0)" ::: "memory")
#define BAR() __builtin_amdgcn_s_barrier()
#define SCHED() __builtin_amdgcn_sched_barrier(0)

#define STAMP_AT(slot) do {} while (0)
#define STAMP_DECL
#define STAMP_OUT() do {} while (0)

struct cpx { float r, i; };
static __device__ __forceinline__ cpx cmul(cpx a, cpx b) { return {a.r * b.r - a.i * b.i, a.r * b.i + a.i * b.r}; }
static __device__ __forceinline__ cpx cfma(cpx a, cpx b, cpx c) {
  return {fmaf(a.r, b.r, fmaf(-a.i, b.i, c.r)), fmaf(a.r, b.i, fmaf(a.i, b.r, c.i))};
}
static __device__ __forceinline__ cpx csel(bool c, cpx a, cpx b) { return {c ? a.r : b.r, c ? a.i : b.i}; }
static __device__ __forceinline__ float silu(float g) { return g * __builtin_amdgcn_rcpf(1.f + __expf(-g)); }
static __device__ __forceinline__ u32 mk_tag(u32 base) { return base * 0x9E3779B1u + 0x7F4A7C15u; }

constexpr int KB8 = 8192, K19 = 19 * KB8;
static __device__ __forceinline__ constexpr int xslot(int c) { return (c & 3) * 16384; }
static __device__ __forceinline__ constexpr int bslot(int c) { return ((c & 3) < 2 ? 16 + (c & 3) : 6 + (c & 3)) * KB8; }
static __device__ __forceinline__ constexpr int wslot(int j) { return (10 + (j + 8) % 9) * KB8; }
static __device__ __forceinline__ constexpr int uslot(int u) { return (u % 9) * 16384; }
static __device__ __forceinline__ constexpr int kind(int u) { return u / 7; }

constexpr int NS = 196;
enum { K_LOC = 0, K_X = 15, K_E1 = K_X + 7, K_AD, K_AD2, K_EA, K_G0a, K_P1 = K_G0a + NG0A, K_P2, K_E, K_S0, K_G0b,
       K_H0 = K_G0b + 24 - NG0A, K_ST0 = K_H0 + 3, K_H0b, K_ST1 = K_H0b + 2, K_CK0, K_CK1, K_T, K_R = K_T + 4, K_C0 = K_R + 4, K_CH, K_TR = K_CH + 9, K_TR2,
       K_STS, K_G1 = K_STS + 2, K_H1 = K_G1 + 24, K_ST2 = K_H1 + 3, K_H1b, K_ST3 = K_H1b + 2, K_CA,
       K_GG, K_SK, K_SC = K_SK + 6, K_RA = K_SC + 7, K_END = K_RA + 4 };
struct Sched { int at[NS][2]; int of[K_END]; int an[NS]; int ao[42]; };
constexpr bool ring_step(int t) { const int j = (t + PD) / 8, k = (t + PD) % 8; return (k == 0 && j >= 2 && j <= 10) || (k == 2 && j >= 1 && j <= 10); }
constexpr int u_piece(int t) {
  const int j = (t + PD) / 8, k = (t + PD) % 8;
  if (j <= 8) return j % 2 == 0 && (k == 4 || k == 6) ? j + (k - 4) / 2 : -1;
  if (j >= 11 && j <= 15) return k == 0 || k == 2 ? 2 * (j - 6) + k / 2 : -1;
  if (j >= 16 && k % 2 == 0 && 4 * j - 44 + k / 2 < 42) return 4 * j - 44 + k / 2;
  return -1;
}
constexpr Sched mk_sched() {
  Sched s = {};
  for (int t = 0; t < NS; ++t) s.at[t][0] = s.at[t][1] = s.an[t] = -1;
  int t = 1, n = 0;
  for (int i = 0; i < K_END; ++i) {
    if (t >= TD && n == 1 && i < K_GG && i != K_H1) n = 2; else { ++t; n = 1; }
    while (ring_step(t) && (t < RT0 || t >= RT1)) ++t;
    if (i == K_G0a && t < 30) t = 30;
    if (i == K_H0 && t < 58) t = 58;
    if (i == K_G1 && t < 86) t = 86;
    if (i == K_H1 && t < 113) t = 113;
    if (i == K_GG && t < TG) t = TG;
    if (i == K_SC && t < 170) t = 170;
    s.of[i] = t;
    s.at[t][n - 1] = i;
  }
  t = TC0;
  for (int i = 0; i < 42; ++i) {
    if (i == 14 && t < TC1) t = TC1;
    if (i == 28 && t < TC2) t = TC2;
    ++t;
    if (t < 112 + 2 * i - LEAD) t = 112 + 2 * i - LEAD;
    while (u_piece(t) >= 0 || t == s.of[K_STS] || t == s.of[K_STS + 1] || t == s.of[K_ST2] || t == s.of[K_ST3]) ++t;
    s.an[t] = i; s.ao[i] = t;
  }
  return s;
}
constexpr Sched SC = mk_sched();
constexpr int bar_after(int t) { while ((t + PD) % 8 != 0) ++t; return t; }
constexpr int T_RD = bar_after(SC.of[K_E1] + 1), T_LB = SC.of[K_CK0] - LBD;
constexpr int T_F0 = bar_after(SC.of[K_ST1] + FD0), T_F1 = bar_after(SC.of[K_STS + 1] + FD1), T_F2 = bar_after(SC.of[K_ST3] + FD2);
constexpr bool a_ok() { for (int i = 0; i < 42; ++i) if (SC.ao[i] >= 112 + 2 * i) return false; return true; }
static_assert(SC.of[K_G0a] >= 30 && SC.of[K_P1] >= T_RD + 2 && SC.of[K_H0] >= 58 && SC.of[K_G1] >= 86 && SC.of[K_AD2] < 108 && SC.of[K_CA] < 136 &&
              SC.of[K_EA] < TEP && SC.of[K_END - 1] < NS && T_F0 < TC0 && T_F1 < TC1 && T_F2 < TC2 && TD > 86 && a_ok(), "schedule");

static __device__ __forceinline__ void spare_job(const Params& p, int si, int xcd) {
  const int tid = threadIdx.x, lane = tid & 63, w = tid >> 6;
  const int sidx = si * 8 + xcd;
  const u32 base = __hip_atomic_load((GAS u32*)(p.ws + WS_BASE), RLX_AGENT);
  u32x4 xr[8];
  const int lc = lane < 56 ? lane : 55;
#pragma unroll
  for (int i = 0; i < 8; ++i) xr[i] = *(const u32x4*)(p.x + (size_t)(sidx * 64 + w * 8 + i) * D + lc * 8);
  const u32x4 gwv = *(const u32x4*)(p.gw + lc * 8);
  const u32 tag = mk_tag(base);
  float res = 0.f;
#pragma unroll
  for (int i = 0; i < 8; ++i) {
    float s = 0.f;
#pragma unroll
    for (int j = 0; j < 4; ++j) s += bflo(xr[i][j]) * bflo(gwv[j]) + bfhi(xr[i][j]) * bfhi(gwv[j]);
    if (lane >= 56) s = 0.f;
#pragma unroll
    for (int o = 32; o > 0; o >>= 1) s += bperm((lane ^ o) * 4, s);
    if (lane == i) res = s;
  }
  asm volatile("" :: "v"(tag));
  BAR();
  if (lane < 8)
    __hip_atomic_store((GAS u64*)(p.ws + WS_G) + sidx * 64 + w * 8 + lane,
                       ((u64)tag << 32) | (u64)f2u(1.f / (1.f + __expf(-res))), RLX_AGENT);
  if (sidx < 28 && tid < 256) st16<0>((u32)(sidx * 8192 + tid * 16), u32x4{0u, tag, 0u, tag}, p.ws + WS_GRAN);
  const int rr = lane >> 3, cp = lane & 7;
  u32x4 acc = {0, 0, 0, 0};
  for (int m = 0; m < 2; ++m) {
    const char* src = (const char*)(m ? p.w3 : p.w1) + (7 * (xcd >> 2)) * 256 + 128 + cp * 16 + (size_t)(si * 112 + rr) * WROW;
    u32x4 v[13];
#pragma unroll
    for (int i = 0; i < 13; ++i) { int it = w + 8 * i; it = it < 98 ? it : 97; v[i] = *(const u32x4*)(src + (it % 7) * 256 + (size_t)(it / 7) * 8 * WROW); }
#pragma unroll
    for (int i = 0; i < 13; ++i) acc ^= v[i];
  }
  {
    u32x4 v[10];
#pragma unroll
    for (int i = 0; i < 10; ++i) {
      int it = sidx * 74 + w + 8 * i; it = it < 2352 ? it : 2351;
      const char* src = it < 1568 ? (const char*)p.w2 + (size_t)it * 1024
                                  : (it < 1960 ? (const char*)p.cre + (size_t)(it - 1568) * 1024 : (const char*)p.cim + (size_t)(it - 1960) * 1024);
      v[i] = *(const u32x4*)(src + lane * 16);
    }
#pragma unroll
    for (int i = 0; i < 10; ++i) acc ^= v[i];
  }
  asm volatile("" :: "v"(acc));
}

template <int LO, int HI> struct LB {
  u32 b[3];
  __device__ __forceinline__ void set(u32 a) {
#pragma unroll
    for (int i = LO; i <= HI; ++i) { b[i] = a + 65536u * i; PIN(b[i]); }
  }
  __device__ __forceinline__ LAS char* at(int off) const { return (LAS char*)(uintptr_t)b[off >> 16] + (off & 65535); }
};

__global__ NOPK __launch_bounds__(512, 2) void fused(Params p) {
  __shared__ __attribute__((aligned(1024))) char smem_[163840];
  LAS char* smem = (LAS char*)smem_;
  const int tid = threadIdx.x, lane = tid & 63;
  const int w = __builtin_amdgcn_readfirstlane(tid >> 6);
  const int xcd = blockIdx.x & 7, idx = blockIdx.x >> 3;
  STAMP_DECL;
  STAMP_AT(0);
  if (idx >= 28) { spare_job(p, idx - 28, xcd); STAMP_AT(19); STAMP_OUT(); return; }
  const int nt = idx % 7, mt = 4 * (xcd & 3) + idx / 7, cb = 7 * (xcd >> 2) + nt;
  const int rb = w & 3, chh = w >> 2, cbv = 2 * cb + chh;
  const int rot = ROT1, rot2 = ROT2;
  int vm = 0;

  const int ch = lane & 15, G = lane >> 4;
  u32 q_re, q_im, q_dt;
  u32 basew = *(const __attribute__((address_space(4))) u32*)(p.ws + WS_BASE);
  {
    const u32 o = (u32)(cb * 32 + (lane & 31)) * 2;
    asm volatile("s_cmp_gt_u32 %7, 3\n\ts_cbranch_scc1 1f\n\t"
                 "global_load_ushort %0, %3, %4\n\tglobal_load_ushort %1, %3, %5\n\tglobal_load_ushort %2, %3, %6\n1:"
                 : "=&v"(q_re), "=&v"(q_im), "=&v"(q_dt)
                 : "v"(o), "s"(p.lre), "s"(p.lim), "s"(p.ldt), "s"(w) : "memory", "scc");
  }
  const int mP = vm;

  const int rr = lane >> 3, cp = lane & 7;
  const u32 xrow = (u32)(mt * 128 + (w >> 1) * 32 + 16 * (rr >> 2) + 8 * (w & 1) + (rr & 3)) * XROW;
  const u32 xo0 = xrow + (u32)((cp ^ (rr >> 1)) * 16), xo1 = xrow + (u32)((cp ^ (4 + (rr >> 1))) * 16) + 4 * XROW;
  const u32 bo = (u32)(cb * 32 + 16 * (w >> 2) + 8 * (w & 1) + rr) * XROW + (u32)((cp ^ (4 * (w & 1) + (rr >> 1))) * 16);
  const char* bsrc = (const char*)((w & 2) ? p.bim : p.bre);
  const u32 lds0 = (u32)(uintptr_t)smem;
  const u32 wb1 = lds0 + w * 1024, wb2 = lds0 + w * 2048;
  u32 ck[7], cu[7];
#pragma unroll
  for (int i = 0; i < 7; ++i) { const int c = i + rot, d = i + rot2; ck[i] = c >= 7 ? c - 7 : c; cu[i] = d >= 7 ? d - 7 : d; }
  int mX[7], mW[28], mU[21];
  auto issue_x = [&](auto c_, auto i_) AINL {
    constexpr int c = c_.v, i = i_.v;
    if constexpr (i < 2) glds<xslot(c) + i * 1024, 0>(wb2, ck[c] * 128, i ? xo1 : xo0, p.x);
    else glds<bslot(c), 0>(wb1, ck[c] * 128, bo, bsrc);
    vm += 1; mX[c] = vm;
  };
  sfor<0, 9>([&](auto n_) AINL { issue_x(IC<n_.v / 3>{}, IC<n_.v % 3>{}); if constexpr (n_.v == 2) BAR(); });
  u32 wo = (u32)(w * 8 + rr) * WROW + cb * 256 + (u32)((cp ^ (((rr >> 1) & 1) << 2)) * 16);
  auto issue_w = [&](auto j_) AINL {
    constexpr int j = j_.v;
    glds<wslot(j), (j / 14) * 128>(wb1, ck[j % 7] * (64 * WROW), wo, (j / 7) & 1 ? p.w3 : p.w1);
    vm += 1; mW[j] = vm;
  };
  u32 fo0 = (u32)(128 * (w >> 2) + 32 * ((w >> 1) & 1) + 16 * (w & 1) + rr) * XROW + nt * 128 + (u32)((cp ^ (((rr >> 1) & 1) << 2)) * 16);
  u32 co0 = (u32)(64 * nt + 16 * (w & 3) + rr) * XROW + (u32)((cp ^ (rr >> 1)) * 16);
  const char* csrc = (const char*)((w >> 2) ? p.cim : p.cre);
  auto issue_u = [&](auto u_, auto h_) AINL {
    constexpr int u = u_.v, h = h_.v;
    if constexpr (kind(u) != 1) glds<uslot(u) + h * 1024, 0>(wb2, cu[u % 7] * (256 * XROW) + (kind(u) * 32 + h * 8) * XROW, fo0, p.w2);
    else glds<uslot(u) + h * 1024, 0>(wb2, cu[u % 7] * 128 + h * 8 * XROW, co0 ^ (u32)(64 * h), csrc);
    vm += 1; mU[u] = vm;
  };
  *(LAS u32*)(smem + K19 + tid * 4) = 0u;
  cpx lam, coef, lam8, lam32, lam128, mq, m8, mw, p512;

  const int r = lane & 31, hk = lane >> 5, q = (lane & 15) >> 2, pp = lane & 3;
  const u32 rowpart = (u32)(r * 128 + ((hk ^ ((r >> 1) & 7)) * 16));
  const u32 wa = (u32)((8 * hk + q) * 128 + 64 * (chh ^ ((q >> 1) & 1)) + 32 * (G & 1) + 16 * (pp & 1) + 8 * (pp >> 1));
  LB<1, 2> WA; WA.set(lds0 + wa);
  u32 xs[4];
#pragma unroll
  for (int s = 0; s < 4; ++s) { xs[s] = lds0 + (u32)(rb * 4096) + (rowpart ^ (u32)(s << 5)); PIN(xs[s]); }
  u32 bs[2][4];
#pragma unroll
  for (int s = 0; s < 4; ++s) {
    bs[0][s] = xs[s] + (u32)(chh * 4096 - rb * 4096 + bslot(0)); bs[1][s] = xs[s] + (u32)(chh * 4096 - rb * 4096 + bslot(2));
    PIN(bs[0][s]); PIN(bs[1][s]);
  }
  const f32x16 zero16 = {0.f, 0.f, 0.f, 0.f, 0.f, 0.f, 0.f, 0.f, 0.f, 0.f, 0.f, 0.f, 0.f, 0.f, 0.f, 0.f};
  u32x4 xr[28], wf[PD + 1];
  f32x16 as, acc[4];
  u32 tw, tr, lbo, sw_, sr_;
  f32x2 cq[9];
  const int kq = w & 3, th = w >> 2, rt = 2 * mt + (xcd >> 2);
  const char* Hw = p.ws + WS_H + (size_t)((mt * 4 + rb) * 28 + cbv) * 6144 + 2048;
  const char* Ha = p.ws + WS_H + (size_t)((2 * rt + th) * 28 + kq) * 6144 + 2048;
  const char* flagp = p.ws + WS_FLAG + mt * FM;
  const char* granp = p.ws + WS_GRAN + cbv * 8192;
  u32 l16o = (u32)lane * 16, pollo = (u32)(2 * (lane < 7 ? lane : 6) + (kq >> 1)) * FC;
  u32 fa_ = lds0 + (u32)((32 * kq + 8 * hk + q) * 128 + 64 * ((q >> 1) & 1) + 32 * (G & 1) + 8 * pp);
  auto rdw = [&](int t) AINL { return rdtr(WA.at(wslot(t >> 2) + (t & 3) * 2048)); };

  tw = lds0 + K19 + (u32)(2048 + lane * 8 + (G == 3 ? ((chh * 8 + 3 + rb) * 16 - 48) * 8 - 2048 : 0));
  tr = lds0 + K19 + (u32)(((chh * 8 + rb) * 16 + ch) * 8);
  lbo = (u32)((mt + 4 * G) * 256 + ch * 16);
  sw_ = lds0 + K19 + (u32)(w * 1024 + ch * 64 + ((G ^ ((ch >> 1) & 3)) * 16));
  sr_ = lds0 + K19 + (u32)(w * 1024 + (8 * hk + q) * 64 + (((2 * (pp & 1) + (G & 1)) ^ (q >> 1)) * 16) + 8 * (pp >> 1));
  PIN(tw); PIN(tr); PIN(lbo); PIN(sw_); PIN(sr_);
  PIN(wo); PIN(fo0); PIN(co0); PIN(pollo); PIN(l16o); PIN(fa_); PINS(Hw); PINS(Ha); PINS(flagp); PINS(granp); PINS(csrc);
#pragma unroll
  for (int i = 0; i < 7; ++i) PINS(cu[i]);
  SCHED();
  asm volatile("s_waitcnt vmcnt(%3)" : "+v"(q_re), "+v"(q_im), "+v"(q_dt) : "n"(vm - mP) : "memory");
  STAMP_AT(1);
  u32 tag = mk_tag(basew);
  PINS(tag);

  constexpr int CT = K19 + 4096;
  if (w < 4) {
    const int pi = 2 * w + (lane >> 5);
    const float n = u2f((u32)(127 + (pi == 0 ? 0 : (pi < 6 ? pi + 2 : pi + 3))) << 23);
    const float dt = bfhi(pk2(0.f, expf(bf2f(q_dt))));
    const float a = bf2f(q_re), b = bf2f(q_im);
    const float m = expf(n * (dt * a));
    float sn, cs; sincosf(n * (dt * b), &sn, &cs);
    LAS f32x2* ct = (LAS f32x2*)(smem + CT) + (lane & 31);
    ct[32 + 32 * pi] = f32x2{m * cs, m * sn};
    if (w == 0) {
      const float nr = m * cs - 1.f, ni = m * sn, inv = 1.f / (a * a + b * b);
      ct[pi * 288] = f32x2{(nr * a + ni * b) * inv, (ni * a - nr * b) * inv};
    }
  }

  STAMP_AT(2);
  u32x4 bq[2][4];
  auto xmm = [&](int c, int s) AINL { if (c >= 0) { as = mfma(xr[4 * c + s], bq[c & 1][s], c + s == 0 ? zero16 : as); PIN(as); } SCHED(); };
  sfor<0, 7>([&](auto c_) AINL {
    constexpr int c = c_.v;
    WAIT_L0(); WV(vm - mX[c]); BAR();
    if constexpr (c == 0) STAMP_AT(3);
    if constexpr (c == 3) STAMP_AT(4);
    SCHED();
    auto piece = [&](auto i_) AINL {
      if constexpr (c < 4) issue_x(IC<c + 3>{}, i_); else issue_w(IC<3 * c - 12 + i_.v>{});
      SCHED();
    };
    piece(IC<0>{});
    xmm(c - 1, 0);
#pragma unroll
    for (int s = 0; s < 4; ++s) {
      xr[4 * c + s] = *(LAS u32x4*)((LAS char*)(uintptr_t)xs[s] + xslot(c));
      bq[c & 1][s] = *(LAS u32x4*)((LAS char*)(uintptr_t)bs[(c >> 1) & 1][s] + (c & 1) * KB8);
    }
    SCHED();
    xmm(c - 1, 1);
    if constexpr (c == 1) {
#pragma unroll
      for (int i = 0; i < 9; ++i) cq[i] = *((LAS f32x2*)(smem + CT) + 32 * i + 16 * chh + ch);
    }
    if constexpr (c == 2) {
      auto cw = [&](int i) AINL { return cpx{cq[i][0], cq[i][1]}; };
      const cpx one = {1.f, 0.f};
      coef = cw(0); lam = cw(1); lam8 = cw(2); lam32 = cw(4); lam128 = cw(6);
      mq = csel(G & 1, lam8, one);
      m8 = cmul(mq, csel(G & 2, cw(3), one));
      mw = cmul(m8, cmul(csel(rb & 1, lam32, one), csel(rb & 2, cw(5), one)));
      p512 = csel(mt - 13 + 4 * G >= 0, cmul(csel(G & 1, one, cw(7)), csel(G & 2, one, cw(8))), cpx{0.f, 0.f});
      PINC(lam); PINC(coef); PINC(lam8); PINC(lam32); PINC(lam128); PINC(mq); PINC(m8); PINC(mw); PINC(p512);
    }
    SCHED();
    piece(IC<1>{}); xmm(c - 1, 2); xmm(c - 1, 3); piece(IC<2>{});
    if constexpr (c == 6) {
      WV(vm - mW[1]); BAR();
#pragma unroll
      for (int t = 0; t < PD; ++t) wf[t] = rdw(t);
      SCHED();
#pragma unroll
      for (int s = 0; s < 4; ++s) xmm(6, s);
    }
    SCHED();
  });
  STAMP_AT(5);

  u32 hp[8], hq[4], pf[3];
  float sg[16], ex[16];
  cpx uu[8], e, Q, P, S0, g1, g2, g3, T;
  f32x2 wg[3];
  u32x4 lb[4], sf[2];
  int mH0 = 0, mS = 0, mH1 = 0, mLB = 0, mF[3];
  auto issue_lb = [&](auto i_) AINL {
    u32x4& d = lb[i_.v]; const u32 o = lbo; const char* gp = granp;
    asm volatile("global_load_dwordx4 %0, %1, %2 offset:%3 sc1" : "=v"(d) : "v"(o), "s"(gp), "n"(i_.v * 256) : "memory");
    vm += 1; mLB = vm;
  };
  auto reissue_lb = [&]() AINL {
    asm volatile("global_load_dwordx4 %0, %4, %5 sc1\n\tglobal_load_dwordx4 %1, %4, %5 offset:256 sc1\n\t"
                 "global_load_dwordx4 %2, %4, %5 offset:512 sc1\n\tglobal_load_dwordx4 %3, %4, %5 offset:768 sc1\n\ts_waitcnt vmcnt(0)"
                 : "+v"(lb[0]), "+v"(lb[1]), "+v"(lb[2]), "+v"(lb[3]) : "v"(lbo), "s"(granp) : "memory");
  };
  auto put_flag = [&](auto R_) AINL {
    u32 a, v; const int w_ = w; const u32 tg = tag, fo = (u32)cb * FC; const char* fp = flagp;
    asm volatile("s_cmp_lg_u32 %2, 0\n\ts_cbranch_scc1 1f\n\ts_mov_b64 exec, 1\n\tv_mov_b32 %0, %3\n\tv_mov_b32 %1, %4\n\t"
                 "global_store_dword %0, %1, %5 offset:%6 sc1\n\ts_mov_b64 exec, -1\n1:"
                 : "=&v"(a), "=&v"(v) : "s"(w_), "s"(fo), "s"(tg), "s"(fp), "n"(R_.v * FR) : "memory", "scc");
  };
  auto poll = [&](auto R_) AINL {
    constexpr int R = R_.v;
    asm volatile("global_load_dword %0, %1, %2 offset:%3 sc1" : "=v"(pf[R]) : "v"(pollo), "s"(flagp), "n"(R * FR) : "memory");
    vm += 1; mF[R] = vm;
  };
  auto check = [&](auto R_) AINL {
    constexpr int R = R_.v;
    const u32 po = pollo, tg = tag; const char* fp = flagp; u32 n; u32& v = pf[R]; const int cnt = vm - mF[R];
    asm volatile("s_waitcnt vmcnt(%6)\n\ts_movk_i32 %1, %7\n"
                 "1:\n\tv_cmp_ne_u32 vcc, %4, %0\n\ts_cbranch_vccz 2f\n\ts_sleep 1\n\t"
                 "global_load_dword %0, %2, %3 offset:%5 sc1\n\ts_waitcnt vmcnt(0)\n\ts_sub_u32 %1, %1, 1\n\ts_cbranch_scc0 1b\n2:"
                 : "+v"(v), "=&s"(n) : "v"(po), "s"(fp), "s"(tg), "n"(R * FR), "n"(cnt), "n"(SPIN_CAP) : "memory", "scc", "vcc");
  };
  auto silu_task = [&](const f32x16& g, int n) AINL {
    const int a = 2 * (n / 3), b = a + 1;
    if (n % 3 == 0) {
      const float m = g[a] * -1.44269504f;
      ex[b] = g[b] * -1.44269504f; PIN(ex[b]);
      if (a) { sg[a - 1] = g[a - 1] * ex[a - 1]; PIN(sg[a - 1]); }
      ex[a] = __builtin_amdgcn_exp2f(m); PIN(ex[a]);
    } else if (n % 3 == 1) {
      const float d = 1.f + ex[a];
      ex[b] = __builtin_amdgcn_exp2f(ex[b]); PIN(ex[b]);
      ex[a] = __builtin_amdgcn_rcpf(d); PIN(ex[a]);
    } else {
      const float d = 1.f + ex[b];
      sg[a] = g[a] * ex[a]; PIN(sg[a]);
      ex[b] = __builtin_amdgcn_rcpf(d); PIN(ex[b]);
    }
  };
  auto h_task = [&](const f32x16& g, const f32x16& up, int n) AINL {
#pragma unroll
    for (int o = 5 * n; o < 5 * n + 5; ++o) {
      if (o == 0) { sg[15] = g[15] * ex[15]; PIN(sg[15]); continue; }
      const int gq = (o - 1) / 6, oo = (o - 1) % 6;
      if (oo < 4) { sg[4 * gq + oo] *= up[4 * gq + oo]; PIN(sg[4 * gq + oo]); }
      else { const int i = 2 * gq + oo - 4; hp[i] = pk2(sg[2 * i], sg[2 * i + 1]); PIN(hp[i]); }
    }
  };
  u32x4 af[21][2];
  int mA[42];
  u32 ao;
  auto issue_a = [&](auto u_, auto h_) AINL {
    constexpr int u = u_.v, h = h_.v, o = (kind(u) == 0 ? -2048 : (kind(u) == 1 ? 2048 : 0)) + 1024 * h;
    if constexpr (h == 0) ao = l16o + cu[u % 7] * (4 * 6144);
    u32x4& d = af[u][h]; const u32 a = ao; const char* hp_ = Ha;
    asm volatile("global_load_dwordx4 %0, %1, %2 offset:%3 sc1" : "=v"(d) : "v"(a), "s"(hp_), "n"(o) : "memory");
    vm += 1; mA[2 * u + h] = vm;
  };
  LB<0, 2> FA0, FA1, CA;
  auto rdf = [&](int t) AINL {
    if (t < 112) return rdw(t);
    const int u = (t - 112) >> 2, i = t & 3;
    if (kind(u) != 1) return rdtr(((i & 1) ? FA1 : FA0).at(uslot(u) + (i >> 1) * 2048));
    return *(LAS u32x4*)CA.at(uslot(u) + (i >> 1) * 8192 + (i & 1) * 4096);
  };
  f32x16 aF[2], aS[2];
  u32x2 gg, xv[2], dv;
  float sk[8], gs[2], gl = 0.f;
  u32 ra[8], go, orow, ocol, eo[2];
  int mE = 0;
  constexpr int RED = 49152;
  u32 bad[4];
  auto lb_bad = [&](int i) AINL { return (lb[i][1] ^ tag) | (lb[i][3] ^ tag); };
  auto task = [&](auto K_) AINL {
    constexpr int K = K_.v;
    if constexpr (K >= K_LOC && K < K_X) {
      if constexpr (K == 0 || K % 2 == 1) {
        constexpr int i = (K + 1) / 2;
        auto sw = __builtin_amdgcn_permlane16_swap(f2u(as[i]), f2u(as[8 + i]), false, false);
        uu[i] = cmul(coef, cpx{u2f(sw[0]), u2f(sw[1])}); PINC(uu[i]);
      } else { e = cfma(lam, K == 2 ? uu[0] : e, uu[K / 2]); PINC(e); }
    }
    if constexpr (K == K_X) { float c = e.r; Q.r = 0.f; swap16(Q.r, c); PIN(Q.r); }
    if constexpr (K == K_X + 1) { float c = e.i; Q.i = 0.f; swap16(Q.i, c); PIN(Q.i); }
    if constexpr (K == K_X + 2) { g1 = cfma(lam8, Q, e); PINC(g1); }
    if constexpr (K == K_X + 3) { g2 = g1; swap16(g2.r, g1.r); swap16(g2.i, g1.i); PINC(g1); }
    if constexpr (K == K_X + 4) { g2.r = 0.f; swap32(g2.r, g1.r); PIN(g2.r); }
    if constexpr (K == K_X + 5) { g2.i = 0.f; swap32(g2.i, g1.i); PIN(g2.i); }
    if constexpr (K == K_X + 6) { Q = cfma(mq, g2, Q); PINC(Q); }
    if constexpr (K == K_E1) {
      e = cfma(lam8, Q, e);
      *(LAS f32x2*)(uintptr_t)tw = f32x2{e.r, e.i};
    }
    if constexpr (K == K_P1) { P = cfma(lam32, cpx{wg[0][0], wg[0][1]}, cpx{wg[1][0], wg[1][1]}); PINC(P); }
    if constexpr (K == K_P2) { P = cfma(lam32, P, cpx{wg[2][0], wg[2][1]}); PINC(P); }
    if constexpr (K == K_E) {
      const cpx E = cfma(lam32, P, e);
      const int rb_ = rb; const u32 o = lbo; const char* gp = granp;
      asm volatile("s_cmp_lg_u32 %0, 3\n\ts_cbranch_scc1 1f\n\ts_mov_b32 exec_lo, 0\n\ts_mov_b32 exec_hi, 0xffff0000\n\t"
                   "global_store_dwordx4 %1, %2, %3 offset:1024 sc1\n\ts_mov_b64 exec, -1\n\ts_nop 0\n1:"
                   :: "s"(rb_), "v"(o), "v"(u32x4{f2u(E.r), tag, f2u(E.i), tag}), "s"(gp) : "memory", "scc");
    }
    if constexpr (K == K_S0) { S0 = cfma(m8, P, Q); PINC(S0); }
    if constexpr (K >= K_G0a && K < K_P1) silu_task(acc[0], K - K_G0a);
    if constexpr (K >= K_G0b && K < K_H0) silu_task(acc[0], K - K_G0b + NG0A);
    if constexpr (K >= K_H0 && K < K_ST0) h_task(acc[0], acc[1], K - K_H0);
    if constexpr (K >= K_H0b && K < K_ST1) h_task(acc[0], acc[1], K - K_H0b + 3);
    if constexpr (K == K_ST0) { sth<-2048>(l16o, u32x4{hp[0], hp[1], hp[2], hp[3]}, Hw); vm += SV; }
    if constexpr (K == K_ST1) { sth<-1024>(l16o, u32x4{hp[4], hp[5], hp[6], hp[7]}, Hw); vm += SV; mH0 = vm; }
    if constexpr (K >= K_G1 && K < K_H1) silu_task(acc[2], K - K_G1);
    if constexpr (K >= K_H1 && K < K_ST2) h_task(acc[2], acc[3], K - K_H1);
    if constexpr (K >= K_H1b && K < K_ST3) h_task(acc[2], acc[3], K - K_H1b + 3);
    if constexpr (K == K_ST2) { sth<0>(l16o, u32x4{hp[0], hp[1], hp[2], hp[3]}, Hw); vm += SV; }
    if constexpr (K == K_ST3) { sth<1024>(l16o, u32x4{hp[4], hp[5], hp[6], hp[7]}, Hw); vm += SV; mH1 = vm; }
    if constexpr (K == K_CK0) {
      asm volatile("s_waitcnt vmcnt(%4)" : "+v"(lb[0]), "+v"(lb[1]), "+v"(lb[2]), "+v"(lb[3]) : "n"(vm - mLB) : "memory");
#pragma unroll
      for (int i = 0; i < 4; ++i) { bad[i] = lb_bad(i); PIN(bad[i]); }
    }
    if constexpr (K == K_CK1) {
      const u32 bd_ = bad[0] | bad[1] | bad[2] | bad[3];
      if (__builtin_expect(!all_lanes(bd_ == 0), 0)) {
        u32 spins = 0;
        do {
          __builtin_amdgcn_s_sleep(1);
          reissue_lb();
        } while (!all_lanes((lb_bad(0) | lb_bad(1) | lb_bad(2) | lb_bad(3)) == 0) && ++spins < SPIN_CAP);
      }
    }
    if constexpr (K >= K_T && K < K_T + 3) {
      T = cfma(lam128, K == K_T ? cpx{u2f(lb[0][0]), u2f(lb[0][2])} : T, cpx{u2f(lb[K - K_T + 1][0]), u2f(lb[K - K_T + 1][2])}); PINC(T);
    }
    if constexpr (K == K_T + 3) { T = cmul(p512, T); PINC(T); }
    if constexpr (K >= K_R && K < K_C0) {
      float& v = (K - K_R) & 1 ? T.i : T.r;
      float c = v;
      if constexpr (K - K_R < 2) swap16(v, c); else swap32(v, c);
      v += c; PIN(v);
    }
    if constexpr (K == K_C0) { T = cfma(mw, T, S0); PINC(T); }
    if constexpr (K >= K_CH && K < K_TR) {
      constexpr int i = K - K_CH;
      if constexpr (i < 8) { T = cfma(lam, T, uu[i]); PINC(T); uu[i] = T; }
      if constexpr (i >= 2 && i % 2 == 0) { hp[i / 2 - 1] = pk2(uu[i - 2].r, uu[i - 1].r); PIN(hp[i / 2 - 1]); }
      if constexpr (i >= 3 && i % 2 == 1) { hq[i / 2 - 1] = pk2(-uu[i - 3].i, -uu[i - 2].i); PIN(hq[i / 2 - 1]); }
      if constexpr (i == 8) { hq[3] = pk2(-uu[6].i, -uu[7].i); PIN(hq[3]); tr = sr_ ^ 32u; PIN(tr); }
    }
    if constexpr (K == K_TR || K == K_TR2) {
      constexpr int f = K == K_TR2;
      *(LAS u32x4*)(uintptr_t)sw_ = f ? u32x4{hq[0], hq[1], hq[2], hq[3]} : u32x4{hp[0], hp[1], hp[2], hp[3]};
      asm volatile("" ::: "memory");
      const u32x2 lo = __builtin_bit_cast(u32x2, __builtin_amdgcn_ds_read_tr16_b64_v4i16((LAS s16x4*)(uintptr_t)sr_));
      const u32x2 hi = __builtin_bit_cast(u32x2, __builtin_amdgcn_ds_read_tr16_b64_v4i16((LAS s16x4*)((LAS char*)(uintptr_t)tr + 256)));
      asm volatile("" ::: "memory");
      sf[f] = u32x4{lo[0], lo[1], hi[0], hi[1]};
    }
    if constexpr (K == K_AD) FA0.set(fa_);
    if constexpr (K == K_AD2) FA1.set(fa_ ^ 64u);
    if constexpr (K == K_STS) { sth<2048>(l16o, sf[0], Hw); vm += SV; }
    if constexpr (K == K_STS + 1) { sth<3072>(l16o, sf[1], Hw); vm += SV; mS = vm; }
    if constexpr (K == K_CA) CA.set(lds0 + (u32)(r * 128 + (((2 * kq + hk) ^ ((r >> 1) & 7)) * 16)));
    if constexpr (K == K_EA) {
      go = (u32)(64 * rt + 32 * th + 16 * ((r >> 2) & 1) + 4 * (r >> 3) + (r & 3)) * 8;
      orow = (u32)(64 * rt + 32 * th + 4 * kq + G); ocol = (u32)(64 * nt + 4 * ch);
      eo[0] = orow * XROW + ocol * 2; eo[1] = ocol * 2;
      PIN(go); PIN(eo[0]); PIN(eo[1]);
    }
    if constexpr (K == K_GG) {
      asm volatile("s_waitcnt vmcnt(%4)" : "+v"(gg), "+v"(xv[0]), "+v"(xv[1]), "+v"(dv) : "n"(vm - mE < 63 ? vm - mE : 63) : "memory");
      if (__builtin_expect(!all_lanes(gg[1] == tag), 0)) {
        u32 spins = 0;
        do {
          __builtin_amdgcn_s_sleep(1);
          asm volatile("global_load_dwordx2 %0, %1, %2 sc1\n\ts_waitcnt vmcnt(0)" : "+v"(gg) : "v"(go), "s"(p.ws + WS_G) : "memory");
        } while (!all_lanes(gg[1] == tag) && ++spins < SPIN_CAP);
      }
      gl = u2f(gg[0]);
      gs[0] = bperm((8 * kq + G) * 4, gl); gs[1] = bperm((8 * kq + 4 + G) * 4, gl);
    }
    if constexpr (K >= K_SK && K < K_SK + 4) {
      constexpr int n = K - K_SK, s = n >> 1, h = n & 1;
      sk[2 * n] = bflo(xv[s][h]) * bflo(dv[h]); sk[2 * n + 1] = bfhi(xv[s][h]) * bfhi(dv[h]);
      PIN(sk[2 * n]); PIN(sk[2 * n + 1]);
    }
    if constexpr (K >= K_SK + 4 && K < K_SC) {
      constexpr int s = K - K_SK - 4;
#pragma unroll
      for (int i = 4 * s; i < 4 * s + 4; ++i) { sk[i] *= gs[s]; PIN(sk[i]); }
      if constexpr (s == 1) { gs[0] = gl + gl; PIN(gs[0]); }
    }
    if constexpr (K >= K_SC && K < K_RA) {
#pragma unroll
      for (int i = 5 * (K - K_SC); i < 5 * (K - K_SC) + 5 && i < 32; ++i) aS[i >> 4][i & 15] *= gs[0];
      PIN(aS[0]); PIN(aS[1]);
      if constexpr (K == K_RA - 1) { gl = 1.f - gl; PIN(gl); }
    }
    if constexpr (K >= K_RA) {
#pragma unroll
      for (int i = 2 * (K - K_RA); i < 2 * (K - K_RA) + 2; ++i) { ra[i] = lds0 + (u32)(RED + w * 8192 + r * 256 + (((2 * i + hk) ^ (r & 15)) * 16)); PIN(ra[i]); }
    }
  };
  sfor<0, NS>([&](auto t_) AINL {
    constexpr int t = t_.v, j = (t + PD) / 8, k = (t + PD) % 8;
    if constexpr (k == 0 && j <= 23) {
      if constexpr (t == T_RD) asm volatile("s_waitcnt lgkmcnt(%0)" :: "n"(2 * (t - SC.of[K_E1] - 1)) : "memory");
      int need = j < 14 ? mW[j < 14 ? 2 * j + 1 : 0] : mU[j >= 14 ? (j == 23 ? 20 : 2 * j - 27) : 0];
      if constexpr (t == T_F0) need = need > mH0 ? need : mH0;
      if constexpr (t == T_F1) need = need > mS ? need : mS;
      if constexpr (t == T_F2) need = need > mH1 ? need : mH1;
      WV(vm - need < 63 ? vm - need : 63); BAR();
      if constexpr (t == T_F0) put_flag(IC<0>{});
      if constexpr (t == T_F1) put_flag(IC<1>{});
      if constexpr (t == T_F2) put_flag(IC<2>{});
    }
    if constexpr (t == 28) STAMP_AT(6);
    if constexpr (t == 56) STAMP_AT(7);
    if constexpr (t == 84) STAMP_AT(8);
    if constexpr (t >= 98 && t % 14 == 0) STAMP_AT(t / 14 + 2);
    SCHED();
    if constexpr (t < 112) { acc[t / 28] = mfma(wf[t % (PD + 1)], xr[t % 28], t % 28 == 0 ? zero16 : acc[t / 28]); PIN(acc[t / 28]); }
    else {
      constexpr int u = (t - 112) >> 2, i = t & 3;
      if constexpr (i % 2 == 0) asm volatile("s_waitcnt vmcnt(%1)" : "+v"(af[u][i >> 1]) : "n"(vm - mA[2 * u + (i >> 1)]) : "memory");
      f32x16& a = (kind(u) == 1 ? aS : aF)[i & 1];
      a = mfma(wf[t % (PD + 1)], af[u][i >> 1], (u == 0 || u == 7) && i < 2 ? zero16 : a); PIN(a);
    }
    SCHED();
    if constexpr (t + PD < NS) wf[(t + PD) % (PD + 1)] = rdf(t + PD);
    SCHED();
    if constexpr (t == T_RD) {
#pragma unroll
      for (int i = 0; i < 3; ++i) wg[i] = *((LAS f32x2*)(uintptr_t)tr + 16 * i);
    }
    if constexpr (SC.at[t][0] >= 0) task(IC<SC.at[t][0]>{});
    if constexpr (SC.at[t][1] >= 0) task(IC<SC.at[t][1]>{});
    SCHED();
    if constexpr (k == 0 && j >= 2 && j <= 10) issue_w(IC<2 * j + 6>{});
    if constexpr (k == 2 && j >= 1 && j <= 10) issue_w(IC<2 * j + 7>{});
    if constexpr (u_piece(t) >= 0) issue_u(IC<u_piece(t) / 2>{}, IC<u_piece(t) % 2>{});
    if constexpr (t >= T_LB && t < T_LB + 8 && (t - T_LB) % 2 == 0) issue_lb(IC<(t - T_LB) / 2>{});
    if constexpr (SC.an[t] >= 0) issue_a(IC<SC.an[t] / 2>{}, IC<SC.an[t] % 2>{});
    if constexpr (t == TC0 - PL) poll(IC<0>{});
    if constexpr (t == TC1 - PL) poll(IC<1>{});
    if constexpr (t == TC2 - PL) poll(IC<2>{});
    if constexpr (t == TC0) check(IC<0>{});
    if constexpr (t == TC1) check(IC<1>{});
    if constexpr (t == TC2) check(IC<2>{});
    if constexpr (t == TEP) { asm volatile("global_load_dwordx2 %0, %1, %2 sc1" : "=v"(gg) : "v"(go), "s"(p.ws + WS_G) : "memory"); vm += 1; }
    if constexpr (t == TEP + 2) { asm volatile("global_load_dwordx2 %0, %1, %2" : "=v"(xv[0]) : "v"(eo[0]), "s"(p.x) : "memory"); vm += 1; }
    if constexpr (t == TEP + 4) { asm volatile("global_load_dwordx2 %0, %1, %2" : "=v"(xv[1]) : "v"(eo[0]), "s"(p.x + 16 * D) : "memory"); vm += 1; }
    if constexpr (t == TEP + 6) { asm volatile("global_load_dwordx2 %0, %1, %2" : "=v"(dv) : "v"(eo[1]), "s"(p.dsk) : "memory"); vm += 1; mE = vm; }
    SCHED();
  });
  STAMP_AT(16);
#pragma unroll
  for (int i = 0; i < 8; ++i) {
    f32x4 o;
#pragma unroll
    for (int jj = 0; jj < 4; ++jj) o[jj] = fmaf(gl, aF[i >> 2][4 * (i & 3) + jj], aS[i >> 2][4 * (i & 3) + jj]);
    *(LAS f32x4*)(uintptr_t)ra[i] = o;
  }
  WAIT_L0(); BAR();
  STAMP_AT(17);
#pragma unroll
  for (int s = 0; s < 2; ++s) {
    const int rw = 8 * kq + 4 * s + G;
    LAS char* a = smem + RED + th * 32768 + rw * 256 + ((ch ^ (rw & 15)) * 16);
    f32x4 v = *(LAS f32x4*)a;
    v = (v + *(LAS f32x4*)(a + 8192)) + (*(LAS f32x4*)(a + 16384) + *(LAS f32x4*)(a + 24576));
    st16<0>((orow + 16 * s) * (D * 4) + ocol * 4,
            u32x4{f2u(v[0] + sk[4 * s]), f2u(v[1] + sk[4 * s + 1]), f2u(v[2] + sk[4 * s + 2]), f2u(v[3] + sk[4 * s + 3])}, p.out);
  }
  if (mt == 15 && cb == 0 && w == 0) {
    for (u32 spins = 0; spins < SPIN_CAP; ++spins) {
      const u64 v = __hip_atomic_load((GAS u64*)(p.ws + WS_G) + r * 64, RLX_AGENT);
      if (all_lanes((u32)(v >> 32) == tag)) break;
      __builtin_amdgcn_s_sleep(1);
    }
    if (lane == 0) __hip_atomic_store((GAS u32*)(p.ws + WS_BASE), tag, RLX_AGENT);
  }
  STAMP_AT(18);
  STAMP_OUT();
}

extern "C" void kernel_launch(void* const* d_in, const int* in_sizes, int n_in, void* d_out, int out_size, void* d_ws,
                              size_t ws_size, hipStream_t stream) {
  Params p;
  p.x = (const u16*)d_in[0]; p.lre = (const u16*)d_in[1]; p.lim = (const u16*)d_in[2];
  p.bre = (const u16*)d_in[3]; p.bim = (const u16*)d_in[4]; p.cre = (const u16*)d_in[5]; p.cim = (const u16*)d_in[6];
  p.ldt = (const u16*)d_in[7]; p.dsk = (const u16*)d_in[8];
  p.w1 = (const u16*)d_in[10]; p.w3 = (const u16*)d_in[11]; p.w2 = (const u16*)d_in[12]; p.gw = (const u16*)d_in[13];
  p.out = (float*)d_out; p.ws = (char*)d_ws;
  fused<<<256, 512, 0, stream>>>(p);
}
```

```cpp
#include <hip/hip_runtime.h>
#include <stdint.h>

typedef unsigned short u16;
typedef unsigned int u32;
typedef unsigned long long u64;
typedef short s16x4 __attribute__((ext_vector_type(4)));
typedef __bf16 bf16x8 __attribute__((ext_vector_type(8)));
typedef __bf16 bf16x2 __attribute__((ext_vector_type(2)));
typedef float f32x2 __attribute__((ext_vector_type(2)));
typedef float f32x4 __attribute__((ext_vector_type(4)));
typedef float f32x16 __attribute__((ext_vector_type(16)));
typedef u32 u32x2 __attribute__((ext_vector_type(2)));
typedef u32 u32x4 __attribute__((ext_vector_type(4)));
typedef u32 u32x16 __attribute__((ext_vector_type(16)));
#define LAS __attribute__((address_space(3)))
#define GAS __attribute__((address_space(1)))
#define RLX_AGENT __ATOMIC_RELAXED, __HIP_MEMORY_SCOPE_AGENT
#if defined(__HIP_DEVICE_COMPILE__)
#define NOPK __attribute__((target("no-packed-fp32-ops")))
#else
#define NOPK
#endif

constexpr int D = 448, F = 1792;
constexpr int XROW = D * 2, WROW = F * 2;
constexpr size_t WS_H = 0;
constexpr size_t WS_G = WS_H + (size_t)64 * 28 * 6144;
constexpr size_t WS_GRAN = WS_G + 2048 * 8;
constexpr size_t WS_FLAG = WS_GRAN + 28 * 32 * 256;
constexpr int FR = 256, FC = 4, FM = 1024;
constexpr size_t WS_BASE = WS_FLAG + 16 * FM;
constexpr u32 SPIN_CAP = 1u << 14;
#define UJ 0
#define US 2
#define PD 3
#define CPF 1
#define NSH 6
#define NUP 12
#define BAR0 1
#define XR0 3
#define XR1 6
#define ROT 2
#define ROTF 2 * (xcd & 3) + 3 * nt + (mq4 & 1)
#define SEQ0 0
#define SEQ 72354
#define WU_N 4
#define NPRE 0
#define NI0 1
#define NI4 3
#define NI5 2
#define NI6 1
#define TC0 95
#define TC1 119
#define TC2 151
#define PL 9
#define PL0 PL
#define PL1 PL
#define PL2 PL
#define FD0 9
#define FD1 12
#define FD2 12
#define TG 121
#define LEAD 12
#define PA 800
#define PB 1200
#define TU0 129
#define LA 20
#define LS LEAD
#define TT 131

#define B0 1632
#define B1 1632
#define B2 1632
#define B3 1632
#define BH 24
#define BD 32
#define TD 100
#define TLB 40
#define LBD 14
#define TEP 68
#define TE0 59
#define TE1 9
#define TE2 11
#define TE3 25
#define TEA 0
#define TAD 100
#define NR 0
#define TR0 77
#define TR1 79
#define TR2 81
#define TS0 86
#define TS1 89
#define TS2 94
#define TR3 79
#define TS3 89
#define RS 1
#define FWD 1
#define NPE 0
#define ORD 1
#define OSC 1
#define TCA 117
#define GD 1
#define XP 257
#define XQ 135
#define XC 2469
#define CPI 3
#define TP1 170
#define TPW -1
#define TPY 0
#define TPZ 112
#define PYC chh
#define OWN 223
#define STR 1 + mq4
#define PFS 128
#define PS 0
#define WU_T 1
#define CTM 16
#define SD0 1
#define SD1 1
#define SD2 7
#define RL 1
#define TBL 189

struct Params {
  const u16 *x, *bre, *bim, *lre, *lim, *ldt;
  char* ws;
  const u16 *gw, *w1, *w3, *w2, *cre, *cim, *dsk;
  float* out;
  const u16* x2;
};

#define u2f(x) __builtin_bit_cast(float, (u32)(x))
#define f2u(x) __builtin_bit_cast(u32, (float)(x))
static __device__ __forceinline__ float bf2f(u32 v) { return u2f(v << 16); }
static __device__ __forceinline__ float bflo(u32 v) { return u2f(v << 16); }
static __device__ __forceinline__ float bfhi(u32 v) { return u2f(v & 0xFFFF0000u); }
static __device__ __forceinline__ u32 pk2(float a, float b) {
  f32x2 f = {a, b};
  return __builtin_bit_cast(u32, __builtin_convertvector(f, bf16x2));
}
static __device__ __forceinline__ float bperm(int lane4, float v) {
  return u2f((u32)__builtin_amdgcn_ds_bpermute(lane4, (int)f2u(v)));
}
static __device__ __forceinline__ void swap16(float& a, float& b) {
  auto r = __builtin_amdgcn_permlane16_swap(f2u(a), f2u(b), false, false); a = u2f(r[0]); b = u2f(r[1]);
}
static __device__ __forceinline__ void swap32(float& a, float& b) {
  auto r = __builtin_amdgcn_permlane32_swap(f2u(a), f2u(b), false, false); a = u2f(r[0]); b = u2f(r[1]);
}
static __device__ __forceinline__ bool all_lanes(bool c) { return __builtin_amdgcn_ballot_w64(!c) == 0; }
#define PIN(x) asm volatile("" : "+v"(x))
#define PINS(x) asm volatile("" : "+s"(x))
#define DUE(x) asm volatile("" :: "v"(x))
#define PINC(c) asm volatile("" :: "v"((c).r), "v"((c).i))
template <bool FIRST> static __device__ __forceinline__ void mfma(f32x4& c, u32x4 a, u32x4 b) {
  if constexpr (FIRST) asm volatile("v_mfma_f32_16x16x32_bf16 %0, %1, %2, 0" : "=&v"(c) : "v"(a), "v"(b));
  else asm volatile("v_mfma_f32_16x16x32_bf16 %0, %1, %2, %0" : "+v"(c) : "v"(a), "v"(b));
}
static __device__ __forceinline__ u32x4 rdtr(LAS char* a, int d) {
  const u32x2 lo = __builtin_bit_cast(u32x2, __builtin_amdgcn_ds_read_tr16_b64_v4i16((LAS s16x4*)a));
  const u32x2 hi = __builtin_bit_cast(u32x2, __builtin_amdgcn_ds_read_tr16_b64_v4i16((LAS s16x4*)(a + d)));
  return u32x4{lo[0], lo[1], hi[0], hi[1]};
}
template <int LO, int GO> static __device__ __forceinline__ void glds(u32 wb, u32 so, u32 vo, const void* sb) {
  asm volatile("s_add_u32 m0, %0, %1\n\ts_nop 0\n\tglobal_load_lds_dwordx4 %2, %3 offset:%4"
               :: "s"(wb), "n"(LO - GO), "v"(vo), "s"((const char*)sb + so), "n"(GO) : "memory", "scc");
}
template <int LO, int GO> static __device__ __forceinline__ void glds_m0(u32 wb, const char* sb) {
  asm volatile("s_add_u32 m0, %0, %1" :: "s"(wb), "n"(LO - GO), "s"(sb) : "memory", "scc");
}
template <int GO> static __device__ __forceinline__ void glds_go(u32 vo, const char* sb) {
  asm volatile("global_load_lds_dwordx4 %0, %1 offset:%2" :: "v"(vo), "s"(sb), "n"(GO) : "memory");
}
template <int O> static __device__ __forceinline__ void st16(u32 voff, u32x4 d, const void* sb) {
  asm volatile("global_store_dwordx4 %0, %1, %2 offset:%3 sc1\n\ts_nop 1" :: "v"(voff), "v"(d), "s"(sb), "n"(O) : "memory");
}
template <int I> struct IC { static constexpr int v = I; };
template <int I, int N, class Fn> static __device__ __forceinline__ void sfor(Fn&& f) {
  if constexpr (I < N) { f(IC<I>{}); sfor<I + 1, N>(f); }
}
#define AINL __attribute__((always_inline))
#define WV(n) asm volatile("s_waitcnt vmcnt(%0)" :: "n"(n) : "memory")
#define WAIT_L0() asm volatile("s_waitcnt lgkmcnt(0)" ::: "memory")
#define BAR() __builtin_amdgcn_s_barrier()
#define SCHED() __builtin_amdgcn_sched_barrier(0)

struct cpx { float r, i; };
static __device__ __forceinline__ cpx cmul(cpx a, cpx b) { return {a.r * b.r - a.i * b.i, a.r * b.i + a.i * b.r}; }
static __device__ __forceinline__ cpx cfma(cpx a, cpx b, cpx c) {
  return {fmaf(a.r, b.r, fmaf(-a.i, b.i, c.r)), fmaf(a.r, b.i, fmaf(a.i, b.r, c.i))};
}
static __device__ __forceinline__ cpx csq(cpx a) { return {fmaf(a.r, a.r, -(a.i * a.i)), (a.r + a.r) * a.i}; }
static __device__ __forceinline__ cpx csel(bool c, cpx a, cpx b) { return {c ? a.r : b.r, c ? a.i : b.i}; }
static __device__ __forceinline__ float silu(float g) { return g * __builtin_amdgcn_rcpf(1.f + __expf(-g)); }
static __device__ __forceinline__ u32 mk_tag(u32 base) { return base * 0x9E3779B1u + 0x7F4A7C15u; }

constexpr int KB8 = 8192, K19 = 19 * KB8;
static __device__ __forceinline__ constexpr int xslot(int c) { return (c & 3) * 16384; }
static __device__ __forceinline__ constexpr int bslot(int c) { return ((c & 3) < 2 ? 16 + (c & 3) : 6 + (c & 3)) * KB8; }
static __device__ __forceinline__ constexpr int wslot(int j) { return (10 + (j + 8) % 9) * KB8; }
static __device__ __forceinline__ constexpr int uslot(int u) { return (u % 9) * 16384; }
static __device__ __forceinline__ constexpr int kind(int u) { return u / 7; }

constexpr int U0_ = OWN / 100, U1_ = 7 + OWN / 10 % 10, U2_ = 14 + OWN % 10;
static __device__ __forceinline__ constexpr int c_of(int u) { return (u + 7 - (u < 7 ? U0_ : (u < 14 ? U1_ : U2_))) % 7; }

constexpr int NS = 196, NWX = NPRE + NI0 + NI4 + NI5 + NI6;
enum { K_EA, K_AD = K_EA + 2 + (TG < TAD ? 6 : 0), K_CA = K_AD + 4, K_GG = TG < TAD ? K_EA + 2 : K_CA + 1, K_SK, K_RA = K_SK + 4, K_SC = K_EA + 13, K_FW = K_SC + 8,
       K_LOC = K_FW + 8, K_X = K_LOC + 15, K_E1 = K_X + 7, K_P1, K_P2, K_E, K_S0,
       K_G0, K_H0 = K_G0 + 90, K_ST0 = K_H0 + 12, K_ST1 = K_ST0 + 13,
       K_CK, K_T, K_TW, K_TS, K_C0 = K_TS + 3, K_CH, K_PK = K_CH + 14, K_STS = K_PK + 2,
       K_G1 = K_STS + 2, K_H1 = K_G1 + 90, K_ST2 = K_H1 + 12, K_ST3 = K_ST2 + 13, K_END };
constexpr int NU = 24;
struct Sched { int at[NS][NU]; int of[K_END]; int an[NS]; int ao[42]; };
constexpr int bar_after(int t) { while ((t + PD) % 8 != 0) ++t; return t; }
constexpr bool is_bar(int t) { return t == TBL || (t < TBL && (t + PD) % 8 == 0); }
constexpr int w_piece(int t) {
  const int j = (t + PD) / 8, k = (t + PD) % 8;
  if (t <= 6 && t % 2 == 0 && NWX + t / 2 < 9) return NWX + t / 2;
  if (k == 0 && j >= 2 && j <= 10) return 2 * j + 6;
  if (k == 2 && j >= 1 && j <= 10) return 2 * j + 7;
  return -1;
}
constexpr int t_r(int i) { return i == 0 ? TR0 : (i == 1 ? TR1 : (i == 2 ? TR2 : TR3)); }
constexpr int t_s(int i) { return i == 0 ? TS0 : (i == 1 ? TS1 : (i == 2 ? TS2 : TS3)); }
constexpr int u_piece(int t) {
  const int j = (t + PD) / 8, k = (t + PD) % 8;
  for (int i = 0; i < NR; ++i) if (t == t_s(i) || t == t_s(i) + 2) return 18 + 2 * i + (t - t_s(i)) / 2;
  if (j <= 10) return j >= UJ && j <= UJ + 4 * US && (j - UJ) % US == 0 && (k == 4 || k == 6) ? (j - UJ) / US * 2 + (k - 4) / 2 : -1;
  if (j >= 11 && j <= 14) return k == 0 || k == 2 ? 2 * (j - 6) + k / 2 : -1;
  if (t >= TU0 && (t - TU0) % 2 == 0 && 18 + 2 * NR + (t - TU0) / 2 < 42) return 18 + 2 * NR + (t - TU0) / 2;
  return -1;
}
constexpr int TQ = NS - 4 * RL;
constexpr int s_u(int t) { return t >= TQ ? 21 - RL + (t - TQ) % RL : (t - 112) >> 2; }
constexpr int s_c(int t) { return t >= TQ ? (t - TQ) / RL : t & 3; }
constexpr int first_use(int u) { return u >= 21 - RL ? TQ + u - (21 - RL) : 112 + 4 * u; }
constexpr int u_need(int t) {
  int m = -1;
  for (int x = t; x + PD < NS && (x == t || !is_bar(x)); ++x) if (x + PD >= 112 && s_u(x + PD) > m) m = s_u(x + PD);
  return m;
}
constexpr int silu_i(int o) { const int n = o / 5, k = o % 5, i = k == 0 || k == 3 ? n : (k == 2 ? n - 2 : n - 1); return i >= 0 && i < 16 ? i : -1; }
constexpr bool is_store(int K) { return K == K_ST0 || K == K_ST1 || K == K_ST2 || K == K_ST3 || K == K_STS || K == K_STS + 1; }
constexpr int cost(int K) {
  if (K >= K_G1) K -= K_G1 - K_G0;
  if (is_store(K)) return 0;
  if (K >= K_G0 && K < K_H0) return silu_i(K - K_G0) < 0 ? 0 : ((K - K_G0) % 5 >= 3 ? 8 : 4);
  if (K >= K_H0 && K < K_CK) return 4;
  if (K == K_TW || K == K_TS + 2) return 8;
  if (K >= K_CH && K < K_PK) return (K - K_CH) % 7 % 3 == 2 ? 8 : ((K - K_CH) % 7 == 6 ? CTM : 16);
  if (K == K_X || K == K_X + 1 || K == K_CK || (K >= K_TS && K < K_C0) || K == K_AD) return 12;
  if (K == K_X + 4 || K == K_X + 5) return 8;
  if (K == K_E || K == K_CA) return 28;
  if (K == K_GG || K == K_SK || K == K_SK + 1 || K == K_RA) return 24;
  if (K < K_EA + 2) return 20;
  return 16;
}
constexpr int tmin(int K, const Sched& s) {
  if (K == K_EA) return TEA;
  if (K == K_AD) return TAD;
  if (K == K_CA) return TCA;
  if (K >= K_SK && K <= K_RA) return s.of[K - 1] + GD;
  if (K == K_GG) return TG;
  if (K >= K_SC && K < K_FW) return 166 + (K - K_SC) / 2;
  if (K >= K_FW && K < K_LOC) return TQ + ((K - K_FW) / 2 + 1) * RL + 1 + FWD;
  if (K == K_LOC) return 2;
  if (K == K_P1) return bar_after(s.of[K_E1] + 1) + 2;
  if (K == K_G0) return 29;
  if (K == K_H0) return 56;
  if (K == K_H0 + 6) return 57;
  if (K == K_CK) return TLB + LBD;
  if (K == K_TS) return bar_after(s.of[K_TW] + 1) + 2;
  if (K == K_CH + 6 || K == K_CH + 13) return s.of[K - 1] + 1;
  if (K == K_PK + 1) return s.of[K_PK - 1] + 2;
  if (K == K_STS + 1) return s.of[K_STS] + SD1;
  if (K == K_ST1) return s.of[K_ST0] + SD0;
  if (K == K_ST3) return s.of[K_ST2] + SD2;
  if (K == K_G1) return 85;
  if (K == K_H1) return 112;
  if (K == K_H1 + 6) return 113;
  return 0;
}
constexpr int budget(int t, bool vs) {
  if (t >= TD) return BD;
  if (t >= 56 && t < 61) return BH;
  const int b = t < 28 ? B0 : (t < 56 ? B1 : (t < 84 ? B2 : B3));
  return vs ? b % 100 : b / 100;
}
constexpr Sched mk_sched() {
  Sched s = {};
  int nx[5] = {K_EA, K_LOC, K_G0, K_CK, K_G1};
  const int en[5] = {K_LOC, K_G0, K_CK, K_G1, K_END};
  for (int i = 0; i < K_END; ++i) s.of[i] = -1;
  for (int t = 0; t < NS; ++t) {
    bool vs = w_piece(t) >= 0 || u_piece(t) >= 0 || t == TLB;
    int used = 0, n = 0;
    for (int c = 0; c < 5; ++c)
      while (nx[c] < en[c] && tmin(nx[c], s) <= t && n < NU) {
        const int K = nx[c];
        if (is_store(K)) { if (vs) break; vs = true; }
        if (used && used + cost(K) > budget(t, vs)) break;
        used += cost(K); s.of[K] = t; s.at[t][n++] = K; ++nx[c];
      }
    while (n < NU) s.at[t][n++] = -1;
    s.an[t] = -1;
  }
  int t = TC0;
  for (int i = 0; i < 42; ++i) {
    if (i == 14 && t < TC1) t = TC1;
    if (i == 28 && t < TC2) t = TC2;
    ++t;
    const int e = first_use(i >> 1) + 2 * (i & 1) - (i < 14 ? LA : (i < 28 ? LS : LEAD));
    if (t < e) t = e;
    while (u_piece(t) >= 0 || t == s.of[K_STS] || t == s.of[K_STS + 1] || t == s.of[K_ST2] || t == s.of[K_ST3]) ++t;
    s.an[t] = i; s.ao[i] = t;
  }
  return s;
}
constexpr Sched SC = mk_sched();
constexpr int T_RD = bar_after(SC.of[K_E1] + 1), T_RD2 = bar_after(SC.of[K_TW] + 1);
constexpr int T_F0 = bar_after(SC.of[K_ST1] + FD0), T_F1 = bar_after(SC.of[K_STS + 1] + FD1), T_F2 = bar_after(SC.of[K_ST3] + FD2);
constexpr bool sched_ok() {
  for (int i = 0; i < 42; ++i) if (SC.ao[i] >= first_use(i >> 1)) return false;
  for (int i = 0; i < K_END; ++i) if (SC.of[i] < 0 && (i < K_FW || i >= K_LOC)) return false;
  for (int i = 0; i < 4; ++i) if (SC.of[K_AD + i] >= 112 - PD + i) return false;
  for (int t = 109; t < NS; ++t) if (is_bar(t) && u_need(t) >= 9 + NR && TU0 + 4 * (u_need(t) - 9 - NR) + 2 >= t) return false;
  for (int u = 9 + NR; u < 21; ++u) if (bar_after(first_use(u - 9) + 4) > TU0 + 4 * (u - 9 - NR)) return false;
  for (int i = 0; i < NR; ++i) if (t_r(i) < 16 * i + 29 || bar_after(t_r(i) + 3 * RS + 5) > t_s(i)) return false;
  int n = 0;
  for (int t = 0; t < NS; ++t) { n += u_piece(t) >= 0; if (u_piece(t) >= 0 && w_piece(t) >= 0) return false; }
  if (n != 42) return false;
  return true;
}
static_assert(SC.of[K_CA] < 136 && SC.of[K_EA + 1] < TE0 && SC.of[K_EA + 1] < TE1 && SC.of[K_EA + 1] < TE2 && SC.of[K_EA + 1] < TE3 && SC.of[K_S0] < SC.of[K_C0] && SC.of[K_E] < TLB && T_F0 < TC0 && T_F1 < TC1 && T_F2 < TC2 &&
              TU0 >= 117 && TU0 + 46 - 4 * NR < 182 && TQ > 181 && TBL >= 181 && TT > SC.of[K_STS + 1] + 8 && sched_ok(), "schedule");

static __device__ __forceinline__ void spare_job(const Params& p, int si, int xcd) {
  const int tid = threadIdx.x, lane = tid & 63, w = tid >> 6;
  const int sidx = 8 * (xcd & 3) + 2 * si + (xcd >> 2);
  const u32 r0 = (u32)__builtin_amdgcn_s_memrealtime();
  const int w0 = __builtin_amdgcn_readfirstlane(si * 8 + w);
  const u32 lb = (u32)(w0 & 7) * 1024, l16 = (u32)lane * 16;
  auto seg = [&](auto k_) AINL {
    constexpr int k = k_.v;
    if constexpr (k == 5 || k == 6) {
      for (int n = 0; n < 4096; ++n) { if ((u32)__builtin_amdgcn_s_memrealtime() - r0 >= (u32)(k == 5 ? PA : PB)) break; __builtin_amdgcn_s_sleep(4); }
    }
    if constexpr (k >= 7) {
      const u32 vo = (u32)((lane >> 3) * WROW + (lane & 7) * 16);
      for (int it = (k == 7 ? 1568 : (k == 8 ? 784 : 0)) + xcd * 32 + w0; it < 3136; it += 256) {
        const int ps = it / 784, r = it - 784 * ps, g = r / 14;
        glds<0, 0>(lb, (u32)(g * 8 * WROW + (r - 14 * g) * 256 + (ps >> 1) * 128), vo, ps & 1 ? p.w3 : p.w1); WV(WU_N);
      }
    }
    if constexpr (k == 2) {
      for (int it = xcd * 294 + w0; it < xcd * 294 + 294; it += 32) {
        const char* src = it < 1568 ? (const char*)p.w2 : (it < 1960 ? (const char*)p.cre - 1568 * 1024 : (const char*)p.cim - 1960 * 1024);
        glds<0, 0>(lb, (u32)it * 1024, l16, src); WV(WU_N);
      }
    }
    auto pause = [&]() AINL {
      if constexpr (PS > 0)
        for (int n = 0; n < 4096; ++n) { if ((u32)__builtin_amdgcn_s_memrealtime() - r0 - (u32)PS >= (u32)(PA - PS)) break; __builtin_amdgcn_s_sleep(4); }
    };
    auto touch = [&](u32 off, const void* sb) AINL {
      asm volatile("s_mov_b32 m0, %0\n\ts_nop 0\n\tglobal_load_lds_dword %1, %2" :: "s"(lb), "v"(off + (u32)lane * PFS), "s"(sb) : "memory");
      WV(WU_T);
    };
    constexpr int PB_ = 64 * PFS;
    if constexpr (k == 3) {
      for (int it = w0; it < 2 * (401408 / PB_); it += 32) touch((u32)(it >> 1) * PB_, it & 1 ? p.cim : p.cre);
    }
    if constexpr (k == 4) {
      for (int it = w0; it < 14 * (57344 / PB_); it += 32) {
        pause();
        const int bb = it / (57344 / PB_);
        touch((u32)(bb * 57344 + it * PB_ + 57344), p.w2);
      }
    }
  };
  seg(IC<SEQ0 / 10>{}); seg(IC<SEQ0 % 10>{});
  const u32 base = __hip_atomic_load((GAS u32*)(p.ws + WS_BASE), RLX_AGENT);
  u32x4 xr[8];
  const int lc = lane < 56 ? lane : 55;
#pragma unroll
  for (int i = 0; i < 8; ++i) xr[i] = *(const u32x4*)(p.x + (size_t)(sidx * 64 + w * 8 + i) * D + lc * 8);
  const u32x4 gwv = *(const u32x4*)(p.gw + lc * 8);
  const u32 tag = mk_tag(base);
  float res = 0.f;
#pragma unroll
  for (int i = 0; i < 8; ++i) {
    float s = 0.f;
#pragma unroll
    for (int j = 0; j < 4; ++j) s += bflo(xr[i][j]) * bflo(gwv[j]) + bfhi(xr[i][j]) * bfhi(gwv[j]);
    if (lane >= 56) s = 0.f;
#pragma unroll
    for (int o = 32; o > 0; o >>= 1) s += bperm((lane ^ o) * 4, s);
    if (lane == i) res = s;
  }
  asm volatile("" :: "v"(tag));
  BAR();
  if (lane < 8)
    __hip_atomic_store((GAS u64*)(p.ws + WS_G) + sidx * 64 + w * 8 + lane,
                       ((u64)tag << 32) | (u64)f2u(1.f / (1.f + __expf(-res))), RLX_AGENT);
  if (sidx < 28 && tid < 256) st16<0>((u32)(sidx * 8192 + tid * 16), u32x4{0u, tag, 0u, tag}, p.ws + WS_GRAN);
  seg(IC<SEQ / 1000000>{}); seg(IC<SEQ / 100000 % 10>{}); seg(IC<SEQ / 10000 % 10>{}); seg(IC<SEQ / 1000 % 10>{}); seg(IC<SEQ / 100 % 10>{}); seg(IC<SEQ / 10 % 10>{}); seg(IC<SEQ % 10>{});
  WV(0);
  if (sidx == 0 && w == 0) {
    GAS u32* a = (GAS u32*)(p.ws + (lane < 32 ? WS_G + lane * 512 + 4 : WS_FLAG + 15 * FM + FR + (lane < 46 ? lane - 32 : 0) * FC));
    for (u32 spins = 0; spins < SPIN_CAP; ++spins) {
      const u32 v = __hip_atomic_load(a, RLX_AGENT);
      if (all_lanes(v == tag)) break;
      __builtin_amdgcn_s_sleep(16);
    }
    if (lane == 0) __hip_atomic_store((GAS u32*)(p.ws + WS_BASE), tag, RLX_AGENT);
  }
}

template <int LO, int HI> struct LB {
  u32 b[3];
  __device__ __forceinline__ void set(u32 a) {
#pragma unroll
    for (int i = LO; i <= HI; ++i) { b[i] = a + 65536u * i; PIN(b[i]); }
  }
  __device__ __forceinline__ LAS char* at(int off) const { return (LAS char*)(uintptr_t)b[off >> 16] + (off & 65535); }
};

constexpr int x_piece(int c, int i) {
  if (c >= 1 && c <= 3) return 3 * (c + 3) + i;
  if (c == 0 && NUP + i < 12) return NUP + i;
  if (c == 0) i -= 12 - NUP;
  const int n = c == 0 ? NI0 : (c == 4 ? NI4 : (c == 5 ? NI5 : NI6));
  const int o = NPRE + (c > 0 ? NI0 : 0) + (c > 4 ? NI4 : 0) + (c > 5 ? NI5 : 0);
  return i < n ? 100 + o + i : -1;
}
static_assert(12 - NUP + NI0 <= 3, "three pieces per iteration");
static_assert(NWX - NI6 >= 2 && NPRE + NI0 + NI4 <= 7 && NWX - NI6 <= 8 && NWX <= 9 && NWX >= 5, "ring slots 7 and 8 hold B chunks first");

__global__ NOPK __launch_bounds__(512, 2) void fused(Params p) {
  __shared__ __attribute__((aligned(1024))) char smem_[163840];
  LAS char* smem = (LAS char*)smem_;
  const int tid = threadIdx.x;
  int lane = tid & 63;
  const int xcd = blockIdx.x & 7, idx = blockIdx.x >> 3;
  if (idx >= 28) { spare_job(p, idx - 28, xcd); return; }
  u32x16 ka, kb;
  asm volatile("s_load_dwordx16 %0, %2, 0x0\n\ts_load_dwordx16 %1, %2, 0x40"
               : "=&s"(ka), "=&s"(kb) : "s"(__builtin_amdgcn_kernarg_segment_ptr()) : "memory");
  SCHED();
  int w = __builtin_amdgcn_readfirstlane(tid >> 6);
  const int mq4 = (idx * 37) >> 8;
  int nt = idx - 7 * mq4, mt = 4 * (xcd & 3) + mq4, cb = 7 * (xcd >> 2) + nt;
  int oth = ((w >> 1) ^ (xcd >> 2)) & 1, kq = 2 * ((cb ^ oth) & 1) + (w >> 2);
  int vm = 0;
  constexpr int CT = K19 + 4096;
  const u32 lds0 = (u32)(uintptr_t)smem;
  u32 wb1 = lds0 + w * 1024, wb2 = lds0 + w * 2048, wb0 = lds0 + w * 64;
  {
    u64 pc; const u32 n = (u32)(idx * 8 + w) * 1024;
    asm volatile("s_getpc_b64 %0\n.Lfz0:\n\ts_cmp_ge_u32 %1, .Lfz1-.Lfz0\n\ts_cbranch_scc1 1f\n\ts_add_u32 m0, %3, %4\n\ts_andn2_b64 %0, %0, 63\n\t"
                 "global_load_lds_dwordx4 %2, %0\n1:" : "=&s"(pc) : "s"(n), "v"(n + (u32)lane * 16), "s"(wb1), "n"(wslot(6)) : "memory", "scc");
  }
  SCHED();
  u32 xo0, xo1, bo, to;
  {
    const int rr = lane >> 3, cp = lane & 7;
    const u32 xrow = (u32)(mt * 128 + (w >> 1) * 32 + 8 * (rr >> 2) + 4 * (w & 1) + (rr & 3)) * XROW;
    xo0 = xrow + (u32)((cp ^ (rr >> 1)) * 16); xo1 = xrow + (u32)((cp ^ (4 + (rr >> 1))) * 16) + 16 * XROW;
    bo = (u32)(cb * 32 + 16 * (w >> 2) + 8 * (w & 1) + rr) * XROW + (u32)((cp ^ (4 * (w & 1) + (rr >> 1))) * 16);
    to = (u32)(lane * 4 + (w == 3 ? (int)WS_BASE : cb * 64));
  }
  PIN(xo0); PIN(xo1); PIN(bo); PIN(to); PINS(wb0); PINS(wb1); PINS(wb2);
  u32 wo, fo0, fo1, co0, co1, cu[7], ck[7];
  cpx lam, coef, lam8, lam32, mq, m8, mw, p512, lt;
  LB<1, 2> WA[2];
  u32 xs[2], bs[2][2];
  u32x2 idn;
  u32 tw, tr, lbo, l16o, pollo, fa_, ca_, tag, hwo, hao, flo, gro;
  constexpr int NSET = 14;
  auto setup = [&](auto n_) AINL {
    constexpr int n = n_.v < 2 ? n_.v + 7 : (n_.v < 9 ? n_.v - 2 : n_.v);
    PIN(lane); asm volatile("" : "+s"(w), "+s"(mt), "+s"(cb), "+s"(nt), "+s"(kq), "+s"(oth));
    __builtin_assume(lane >= 0 && lane < 64 && w >= 0 && w < 8 && mt >= 0 && mt < 16 && cb >= 0 && cb < 14 && nt >= 0 && nt < 7 && kq >= 0 && kq < 4);
    const int ln = lane, rb = w & 3, chh = w >> 2, cbv = 2 * cb + chh;
    const int ch = ln & 15, G = ln >> 4, hk = ln >> 5, q = (ln & 15) >> 2, pp = ln & 3, rr = ln >> 3, cp = ln & 7;
    if constexpr (n == 0) {
      const u32 rowpart = (u32)(rb * 4096 + ch * 128 + ((G ^ (ch >> 1)) * 16));
#pragma unroll
      for (int s = 0; s < 2; ++s) { xs[s] = lds0 + (rowpart ^ (u32)(s << 6)); PIN(xs[s]); }
    }
    if constexpr (n == 1) {
#pragma unroll
      for (int s = 0; s < 2; ++s) {
        bs[0][s] = xs[s] + (u32)(chh * 4096 - rb * 4096 + bslot(0)); bs[1][s] = xs[s] + (u32)(chh * 4096 - rb * 4096 + bslot(2));
        PIN(bs[0][s]); PIN(bs[1][s]);
      }
    }
    if constexpr (n == 2) {
      l16o = (u32)ln * 16; PIN(l16o);
      ca_ = lds0 + CT + (u32)(32 * chh + 2 * ch); PIN(ca_);
      *(LAS u32*)(smem + K19 + tid * 4) = 0u;
    }
    if constexpr (n == 3) {
      wo = (u32)rr * WROW + (u32)(w * 8 * WROW + cb * 256) + (u32)((cp ^ (2 * (rr & 2) + 2 * (w & 1))) * 16);
      PIN(wo);
    }
    if constexpr (n == 4) {
      fo0 = (u32)rr * XROW + (u32)((128 * (w >> 2) + 16 * (w & 3)) * XROW + nt * 128) + (u32)((cp ^ (rr & 6)) * 16);
      fo1 = fo0 + 8 * XROW;
      PIN(fo0); PIN(fo1);
    }
    if constexpr (n == 5) { fa_ = lds0 + (u32)(4096 * kq + (ln >> 2) * 128 + ((ln >> 3) & 3) * 32 + 8 * pp); PIN(fa_); }
    if constexpr (n == 6) {
      const u32 a = lds0 + (u32)((8 * G + q) * 128 + 64 * (chh ^ (q >> 1)) + 32 * (G & 1) + 8 * pp);
      WA[0].set(a); WA[1].set(a ^ 32u);
    }
    if constexpr (n == 7) {
      cu[0] = cb >> 1; PINS(cu[0]);
#pragma unroll
      for (int i = 1; i < 3; ++i) { const u32 z = cu[i - 1] + (STR); cu[i] = z >= 7 ? z - 7 : z; PINS(cu[i]); }
      const int y = ROTF; ck[0] = y - 7 * ((y * 147) >> 10); PINS(ck[0]);
#pragma unroll
      for (int i = 1; i < 7; ++i) { ck[i] = ck[i - 1] == 6 ? 0 : ck[i - 1] + 1; PINS(ck[i]); }
    }
    if constexpr (n == 8) {
#pragma unroll
      for (int i = 3; i < 7; ++i) { const u32 z = cu[i - 1] + (STR); cu[i] = z >= 7 ? z - 7 : z; PINS(cu[i]); }
    }
    if constexpr (n == 9) {
      tw = lds0 + K19 + (u32)(5120 + chh * 1024 + ln * 4);
      tr = lds0 + K19 + (u32)((chh * 8 + rb) * 128 + ch * 4);
      PIN(tw); PIN(tr);
    }
    if constexpr (n == 10) {
      hwo = (u32)(WS_H + ((mt * 4 + rb) * 28 + cbv) * 6144 + 2048);
      hao = (u32)(WS_H + ((4 * mt + 2 * (xcd >> 2) + (w & 1)) * 28 + kq) * 6144 + 2048);
      PINS(hwo); PINS(hao);
    }
    if constexpr (n == 11) {
      flo = (u32)(WS_FLAG + mt * FM); gro = (u32)(WS_GRAN + cbv * 8192);
      lbo = (u32)((mt + rb + 4 * G) * 256 + ch * 16);
      PINS(flo); PINS(gro); PIN(lbo);
    }
    if constexpr (n == 12) {
      pollo = (u32)(2 * (ln < 7 ? ln : 6) + (kq >> 1)) * FC;
      co0 = (u32)rr * XROW + (u32)((64 * nt + 16 * rb) * XROW) + (u32)((cp ^ (rr >> 1)) * 16);
      co1 = (co0 ^ 64u) + 8 * XROW;
      PIN(pollo); PIN(co0); PIN(co1);
    }
    if constexpr (n == 13) {
      const u32 one = G == q ? 0x3F80u << (16 * (pp & 1)) : 0u;
      idn = u32x2{pp < 2 ? one : 0u, pp < 2 ? 0u : one}; PIN(idn);
    }
    SCHED();
  };
  sfor<0, NSH>(setup);
  asm volatile("s_waitcnt lgkmcnt(0)" : "+s"(ka), "+s"(kb) :: "memory");
  auto arg = [&](int i) AINL { const u32x16& v = i < 8 ? ka : kb; return (const char*)(((u64)v[2 * (i & 7) + 1] << 32) | (u64)v[2 * (i & 7)]); };
  auto pick = [&](bool c, const char* a, const char* b) AINL { PINS(a); PINS(b); return c ? a : b; };
  const char *px = arg(0), *ws = arg(6), *pw1 = arg(8), *pw3 = arg(9), *pw2 = arg(10), *pdsk = arg(13), *pout = arg(14);
  const char *bsrc, *csrc, *Hw, *Ha, *flagp, *granp;
  int mX[7], mW[28], mU[21];
  auto issue_x = [&](auto n_) AINL {
    constexpr int c = n_.v / 3, i = n_.v % 3;
    if constexpr (i < 2) glds<xslot(c) + i * 1024, 0>(wb2, ck[c] * 128, i ? xo1 : xo0, px);
    else glds<bslot(c), 0>(wb1, ck[c] * 128, bo, bsrc);
    vm += 1; mX[c] = vm;
  };
  auto w_src = [&](int j) AINL { return ((j / 7) & 1 ? pw3 : pw1) + (ROT ? ck[j % 7] : j % 7) * (64 * WROW); };
  auto issue_w = [&](auto j_) AINL {
    constexpr int j = j_.v;
    glds<wslot(j), (j / 14) * 128>(wb1, 0, wo, w_src(j));
    vm += 1; mW[j] = vm;
  };
  u32x4 xr[28], wf[PD + 1];
  f32x4 as[4], acc[4][4];
  auto rdw = [&](int t) AINL { return rdtr(WA[t & 1].at(wslot(t >> 2) + (t & 2) * 2048), 512); };
  issue_x(IC<0>{}); issue_x(IC<1>{});
  bsrc = pick(w & 2, arg(2), arg(1));
  issue_x(IC<2>{});
  {
    const char* ts = pick(w & 2, pick(w & 1, ws, arg(5)), pick(w & 1, arg(4), arg(3)));
    asm volatile("s_cmp_gt_u32 %3, 3\n\ts_cbranch_scc1 1f\n\ts_add_u32 m0, %2, %4\n\ts_mov_b64 exec, 0xffff\n\t"
                 "global_load_lds_dword %0, %1\n\ts_mov_b64 exec, -1\n1:"
                 :: "v"(to), "s"(ts), "s"(wb0), "s"(w), "n"(CT) : "memory", "scc");
  }
  SCHED();
  if (BAR0) BAR();
  sfor<3, NUP + NPRE>([&](auto n_) AINL {
    if constexpr (n_.v < NUP) issue_x(n_); else issue_w(IC<n_.v - NUP>{});
    SCHED();
    if constexpr (NSH + n_.v - 3 < NSET) setup(IC<NSH + n_.v - 3>{});
  });
  sfor<NSH + NUP + NPRE - 3, NSET>(setup);
  const int rb = w & 3, chh = w >> 2, th = w & 1, rt = 2 * mt + (xcd >> 2);
  const int ch = lane & 15, G = lane >> 4, hk = lane >> 5;

  u32 q_re, q_im, q_dt, q_b;
  float ca, cbi, dt, c0, c1, rev, lg;
  constexpr int NCON = 15;
  auto consts = [&](auto n_) AINL {
    constexpr int n = n_.v;
    const cpx one = {1.f, 0.f};
    if constexpr (n == 0) {
      const float x = bf2f(q_dt), t = x * 1.44269502f;
      c0 = fmaf(x, 1.92596303e-8f, fmaf(x, 1.44269502f, -t)) * 0.693147182f; c1 = __builtin_amdgcn_exp2f(t);
      DUE(c0); DUE(c1);
    }
    if constexpr (n == 1) {
      dt = bfhi(pk2(0.f, fmaf(c1, c0, c1)));
      ca = bf2f(q_re); cbi = bf2f(q_im);
      tag = mk_tag(__builtin_amdgcn_readfirstlane(q_b));
      DUE(dt); DUE(ca); DUE(cbi); PINS(tag);
    }
    if constexpr (n == 2) {
      const float a = dt * cbi;
      rev = -fmaf(a, 6.42063833e-9f, fmaf(a, 0.159154937f, -__builtin_rintf(a * 0.159154937f)));
      lg = dt * ca * 1.44269502f; DUE(rev); DUE(lg);
    }
    auto pw0 = [&](float k) AINL { c0 = __builtin_amdgcn_fractf(k * rev); c1 = __builtin_amdgcn_exp2f(k * lg); DUE(c0); DUE(c1); };
    auto pw1 = [&](cpx& z) AINL { z = cpx{c1 * __builtin_amdgcn_cosf(c0), c1 * __builtin_amdgcn_sinf(c0)}; PINC(z); };
    if constexpr (n == 3) { c0 = rev; c1 = __builtin_amdgcn_exp2f(lg); pw1(lam); }
    if constexpr (n == 4) { c0 = lam.r - 1.f; c1 = __builtin_amdgcn_rcpf(fmaf(ca, ca, cbi * cbi)); DUE(c0); DUE(c1); }
    if constexpr (n == 5) { coef = cpx{fmaf(c0, ca, -(lam.i * cbi)) * c1, fmaf(lam.i, ca, c0 * cbi) * c1}; PINC(coef); }
    if constexpr (n == 6) { c0 = 8.f * rev; c1 = __builtin_amdgcn_exp2f(8.f * lg); DUE(c0); DUE(c1); Hw = ws + hwo; PINS(Hw); }
    if constexpr (n == 7) { pw1(lam8); Ha = ws + hao; PINS(Ha); }
    if constexpr (n == 8) { lt = csq(lam8); mq = csel(G & 1, lam8, one); PINC(lt); PINC(mq); }
    if constexpr (n == 9) { lam32 = csq(lt); m8 = csel(G & 2, lt, one); PINC(lam32); PINC(m8); }
    if constexpr (n == 10) { m8 = cmul(mq, m8); PINC(m8); granp = ws + gro; PINS(granp); }
    if constexpr (n == 11) {
      const float k = (float)(8 * G + 32 * rb);
      c0 = k * rev; c1 = __builtin_amdgcn_exp2f(k * lg); DUE(c0); DUE(c1); flagp = ws + flo; PINS(flagp);
    }
    if constexpr (n == 12) { pw1(mw); csrc = pick(chh, arg(12), arg(11)); PINS(csrc); }
    if constexpr (n == 13) pw0((float)(128 * (15 - rb) - 512 * G));
    if constexpr (n == 14) { c1 = mt + rb + 4 * G >= 16 ? c1 : 0.f; pw1(p512); }
    SCHED();
  };

  u32x4 bq[2][4];
  auto xmm = [&](int c, int s) AINL {
    if (c == 0 && s < 4) mfma<true>(as[s], xr[s >> 1], bq[0][s & 1]);
    else if (c >= 0) mfma<false>(as[s & 3], xr[4 * c + (s >> 1)], bq[c & 1][(s >> 2) * 2 + (s & 1)]);
    SCHED();
  };
  sfor<0, 7>([&](auto c_) AINL {
    constexpr int c = c_.v;
    WAIT_L0(); WV(vm - (c == 6 && mW[1] > mX[c] ? mW[1] : mX[c])); BAR();
    SCHED();
    auto piece = [&](auto i_) AINL {
      constexpr int n = x_piece(c, i_.v);
      if constexpr (n >= 100) issue_w(IC<n - 100>{}); else if constexpr (n >= 0) issue_x(IC<n>{});
      SCHED();
    };
    auto rd = [&](int i0, int i1) AINL {
#pragma unroll
      for (int i = i0; i < i1; ++i) {
        const int s = i >> 1;
        if (i & 1) bq[c & 1][s] = *(LAS u32x4*)((LAS char*)(uintptr_t)bs[(c >> 1) & 1][s >> 1] + (c & 1) * KB8 + (s & 1) * 2048);
        else xr[4 * c + s] = *(LAS u32x4*)((LAS char*)(uintptr_t)xs[s >> 1] + xslot(c) + (s & 1) * 2048);
      }
      SCHED();
    };
    auto cs = [&](auto i_) AINL { if constexpr (c >= 1 && i_.v < CPI && CPI * (c - 1) + i_.v < NCON) consts(IC<CPI * (c - 1) + i_.v>{}); };
    if constexpr (c == 0) {
      LAS u16* a = (LAS u16*)(uintptr_t)ca_;
      q_re = a[0]; q_im = a[32]; q_dt = a[64]; q_b = *(LAS u32*)(smem + CT + 192);
    }
    sfor<0, 9>([&](auto m_) AINL {
      constexpr int m = m_.v;
      if constexpr (m == XQ / 100) rd(0, XR0);
      if constexpr (m == XQ / 10 % 10) rd(XR0, XR1);
      if constexpr (m == XP / 100) piece(IC<0>{});
      if constexpr (m == XP / 10 % 10) piece(IC<1>{});
      if constexpr (m == XP % 10) piece(IC<2>{});
      if constexpr (m == XQ % 10) rd(XR1, 8);
      if constexpr (c == 6 && m == 6) {
#pragma unroll
        for (int t = 0; t < PD; ++t) wf[t] = rdw(t);
        SCHED();
      }
      if constexpr (m == XC / 1000) cs(IC<0>{});
      if constexpr (m == XC / 100 % 10) cs(IC<1>{});
      if constexpr (m == XC / 10 % 10) cs(IC<2>{});
      if constexpr (m == XC % 10) cs(IC<3>{});
      if constexpr (m < 8) xmm(c - 1, m);
    });
    if constexpr (c == 6) {
#pragma unroll
      for (int s = 0; s < 8; ++s) xmm(6, s);
    }
    SCHED();
  });

  u32 hp[8], pf[3];
  u32x2 hs[2];
  f32x4 tm[4];
  float sg[16];
  cpx uu[8], e, Q, P, S0, g1, g2, T;
  f32x2 wg[3];
  u32x4 lb, sf[2];
  int mH0 = 0, mS = 0, mH1 = 0, mLB = 0, mF[3];
  auto put_flag = [&](auto R_) AINL {
    u32 a, v; const int w_ = w; const u32 tg = tag, fo = (u32)cb * FC; const char* fp = flagp;
    asm volatile("s_cmp_lg_u32 %2, 0\n\ts_cbranch_scc1 1f\n\ts_mov_b64 exec, 1\n\tv_mov_b32 %0, %3\n\tv_mov_b32 %1, %4\n\t"
                 "global_store_dword %0, %1, %5 offset:%6 sc1\n\ts_mov_b64 exec, -1\n1:"
                 : "=&v"(a), "=&v"(v) : "s"(w_), "s"(fo), "s"(tg), "s"(fp), "n"(R_.v * FR) : "memory", "scc");
  };
  auto poll = [&](auto R_) AINL {
    constexpr int R = R_.v;
    asm volatile("global_load_dword %0, %1, %2 offset:%3 sc1" : "=v"(pf[R]) : "v"(pollo), "s"(flagp), "n"(R * FR) : "memory");
    vm += 1; mF[R] = vm;
  };
  auto check = [&](auto R_) AINL {
    constexpr int R = R_.v;
    const u32 po = pollo, tg = tag; const char* fp = flagp; u32 n; u32& v = pf[R]; const int cnt = vm - mF[R];
    asm volatile("s_waitcnt vmcnt(%6)\n\ts_movk_i32 %1, %7\n"
                 "1:\n\tv_cmp_ne_u32 vcc, %4, %0\n\ts_cbranch_vccz 2f\n\ts_sleep 1\n\t"
                 "global_load_dword %0, %2, %3 offset:%5 sc1\n\ts_waitcnt vmcnt(0)\n\ts_sub_u32 %1, %1, 1\n\ts_cbranch_scc0 1b\n2:"
                 : "+v"(v), "=&s"(n) : "v"(po), "s"(fp), "s"(tg), "n"(R * FR), "n"(cnt), "n"(SPIN_CAP) : "memory", "scc", "vcc");
  };
  auto silu_op = [&](const f32x4* g, int o) AINL {
    const int i = silu_i(o), k = o % 5;
    if (i < 0) return;
    if (k == 0) sg[i] = g[i >> 2][i & 3] * -1.44269504f;
    if (k == 1) sg[i] = 1.f + sg[i];
    if (k == 2) sg[i] *= g[i >> 2][i & 3];
    if (k == 3) sg[i] = __builtin_amdgcn_exp2f(sg[i]);
    if (k == 4) sg[i] = __builtin_amdgcn_rcpf(sg[i]);
    DUE(sg[i]);
  };
  auto h_op = [&](const f32x4* up, int o) AINL {
    const int gq = o / 6, oo = o % 6;
    if (oo < 4) { sg[4 * gq + oo] *= up[gq][oo]; DUE(sg[4 * gq + oo]); }
    else { const int i = 2 * gq + oo - 4; hp[i] = pk2(sg[2 * i], sg[2 * i + 1]); DUE(hp[i]); }
  };
  u32x4 af[21][2], uf[NR + 1][4];
  int mA[42];
  u32 ao;
  auto issue_a = [&](auto u_, auto h_) AINL {
    constexpr int u = u_.v, h = h_.v, o = (kind(u) == 0 ? -2048 : (kind(u) == 1 ? 2048 : 0)) + 1024 * h;
    if constexpr (h == 0) ao = l16o + cu[c_of(u)] * (4 * 6144);
    u32x4& d = af[u][h]; const u32 a = ao; const char* hp_ = Ha;
    if constexpr (c_of(u) == 0) {
      const int ot = oth;
      asm volatile("s_cmp_eq_u32 %4, 0\n\ts_cbranch_scc1 1f\n\tglobal_load_dwordx4 %0, %1, %2 offset:%3 sc1\n1:"
                   : "+v"(d) : "v"(a), "s"(hp_), "n"(o), "s"(ot) : "memory", "scc");
    } else {
      asm volatile("global_load_dwordx4 %0, %1, %2 offset:%3 sc1" : "=v"(d) : "v"(a), "s"(hp_), "n"(o) : "memory");
      vm += 1;
    }
    mA[2 * u + h] = vm;
  };
  LB<0, 2> FA[4], CA;
  auto rdf = [&](int t) AINL {
    if (t < 112) return rdw(t);
    const int u = s_u(t), i = s_c(t);
    if (kind(u) != 1) return rdtr(FA[i].at(uslot(u)), 2048);
    const u32x2 re = *(LAS u32x2*)CA.at(uslot(u) + i * 2048);
    asm volatile("" ::: "memory");
    const u32x2 im = *(LAS u32x2*)CA.at(uslot(u) + i * 2048 + KB8);
    return u32x4{re[0], re[1], im[0], im[1]};
  };
  f32x4 aF[8], aS[8];
  u32x2 gg, xv[2], dv;
  float sk[8], gs[2], g2x, gl;
  u32 rv, go, oo, eo[2];
  int mE = 0;
  constexpr int RED = 49152, RW = 8448;
  auto task = [&](auto K_) AINL {
    constexpr int K = K_.v;
    if constexpr (K >= K_LOC && K < K_X) {
      constexpr int n = K - K_LOC;
      if constexpr (n == 0 || n % 2 == 1) {
        constexpr int i = (n + 1) / 2;
        const float br = as[(i >> 2) * 2][i & 3], bi = as[(i >> 2) * 2 + 1][i & 3];
        uu[i] = cpx{fmaf(coef.r, br, coef.i * bi), fmaf(coef.i, br, -(coef.r * bi))}; PINC(uu[i]);
      } else { e = cfma(lam, n == 2 ? uu[0] : e, uu[n / 2]); PINC(e); }
    }
    if constexpr (K == K_X) { float c = e.r; Q.r = 0.f; swap16(Q.r, c); DUE(Q.r); }
    if constexpr (K == K_X + 1) { float c = e.i; Q.i = 0.f; swap16(Q.i, c); DUE(Q.i); }
    if constexpr (K == K_X + 2) { g1 = cfma(lam8, Q, e); PINC(g1); }
    if constexpr (K == K_X + 3) { g2 = g1; swap16(g2.r, g1.r); swap16(g2.i, g1.i); PINC(g1); }
    if constexpr (K == K_X + 4) { g2.r = 0.f; swap32(g2.r, g1.r); DUE(g2.r); }
    if constexpr (K == K_X + 5) { g2.i = 0.f; swap32(g2.i, g1.i); DUE(g2.i); }
    if constexpr (K == K_X + 6) { Q = cfma(mq, g2, Q); PINC(Q); }
    if constexpr (K == K_E1) {
      e = cfma(lam8, Q, e);
      asm volatile("s_mov_b32 m0, %2\n\ts_mov_b32 exec_lo, 0\n\ts_mov_b32 exec_hi, 0xffff0000\n\t"
                   "ds_write_addtid_b32 %0 offset:%3\n\tds_write_addtid_b32 %1 offset:%4\n\ts_mov_b64 exec, -1"
                   :: "v"(e.r), "v"(e.i), "s"(lds0 + (u32)(K19 - 57344 + (chh * 8 + rb) * 128)), "n"(57344 + 192), "n"(57344 + 256) : "memory");
    }
    if constexpr (K == K_P1) { P = cfma(lam32, cpx{wg[0][0], wg[0][1]}, cpx{wg[1][0], wg[1][1]}); PINC(P); }
    if constexpr (K == K_P2) { P = cfma(lam32, P, cpx{wg[2][0], wg[2][1]}); PINC(P); }
    if constexpr (K == K_E) {
      const cpx E = cfma(lam32, P, e);
      const int rb_ = rb; const u32 o = lbo; const char* gp = granp;
      asm volatile("s_cmp_lg_u32 %0, 3\n\ts_cbranch_scc1 1f\n\ts_mov_b32 exec_lo, 0\n\ts_mov_b32 exec_hi, 0xffff0000\n\t"
                   "global_store_dwordx4 %1, %2, %3 offset:256 sc1\n\ts_mov_b64 exec, -1\n\ts_nop 0\n1:"
                   :: "s"(rb_), "v"(o), "v"(u32x4{f2u(E.r), tag, f2u(E.i), tag}), "s"(gp) : "memory", "scc");
    }
    if constexpr (K == K_S0) { S0 = cfma(m8, P, Q); PINC(S0); }
    if constexpr (K >= K_G0 && K < K_H0) silu_op(acc[0], K - K_G0);
    if constexpr (K >= K_H0 && K < K_ST1 && K != K_ST0) h_op(acc[1], K - K_H0 - (K > K_ST0));
    if constexpr (K == K_ST0) { af[U0_][0] = u32x4{hp[0], hp[1], hp[2], hp[3]}; st16<-2048>(l16o, af[U0_][0], Hw); vm += 1; }
    if constexpr (K == K_ST1) { af[U0_][1] = u32x4{hp[4], hp[5], hp[6], hp[7]}; st16<-1024>(l16o, af[U0_][1], Hw); vm += 1; mH0 = vm; }
    if constexpr (K >= K_G1 && K < K_H1) silu_op(acc[2], K - K_G1);
    if constexpr (K >= K_H1 && K < K_ST3 && K != K_ST2) h_op(acc[3], K - K_H1 - (K > K_ST2));
    if constexpr (K == K_ST2) { af[U2_][0] = u32x4{hp[0], hp[1], hp[2], hp[3]}; st16<0>(l16o, af[U2_][0], Hw); vm += 1; }
    if constexpr (K == K_ST3) { af[U2_][1] = u32x4{hp[4], hp[5], hp[6], hp[7]}; st16<1024>(l16o, af[U2_][1], Hw); vm += 1; mH1 = vm; }
    if constexpr (K == K_CK) {
      asm volatile("s_waitcnt vmcnt(%1)" : "+v"(lb) : "n"(vm - mLB) : "memory");
      if (__builtin_expect(!all_lanes(((lb[1] ^ tag) | (lb[3] ^ tag)) == 0), 0)) {
        u32 spins = 0;
        do {
          __builtin_amdgcn_s_sleep(1);
          asm volatile("global_load_dwordx4 %0, %1, %2 sc1\n\ts_waitcnt vmcnt(0)" : "+v"(lb) : "v"(lbo), "s"(granp) : "memory");
        } while (!all_lanes(((lb[1] ^ tag) | (lb[3] ^ tag)) == 0) && ++spins < SPIN_CAP);
      }
    }
    if constexpr (K == K_T) { T = cmul(p512, cpx{u2f(lb[0]), u2f(lb[2])}); PINC(T); }
    if constexpr (K == K_TW) {
      swap16(T.r, T.i); T.r += T.i;
      asm volatile("s_mov_b32 m0, %1\n\ts_nop 0\n\tds_write_addtid_b32 %0 offset:%2"
                   :: "v"(T.r), "s"(lds0 + (u32)(K19 - 57344 + w * 256)), "n"(57344 + 5120) : "memory");
    }
    if constexpr (K == K_TS) { T.r = (wg[0][0] + wg[0][1]) + (wg[1][0] + wg[1][1]); DUE(T.r); }
    if constexpr (K == K_TS + 1) { T.i = T.r; swap32(T.r, T.i); T.r += T.i; DUE(T.r); }
    if constexpr (K == K_TS + 2) { T.i = T.r; swap16(T.r, T.i); PINC(T); }
    if constexpr (K == K_C0) { T = cfma(mw, T, S0); PINC(T); }
    if constexpr (K >= K_CH && K < K_PK) {
      constexpr int f = (K - K_CH) / 7, m = (K - K_CH) % 7, i = 4 * f + 2 * (m / 3) + m % 3;
      if constexpr (m == 6) {
        asm volatile("v_mfma_f32_16x16x16_bf16 %0, %2, %4, 0\n\tv_mfma_f32_16x16x16_bf16 %1, %3, %4, 0"
                     : "=&v"(tm[2 * f]), "=&v"(tm[2 * f + 1]) : "v"(hs[0]), "v"(hs[1]), "v"(idn));
      } else if constexpr (m % 3 < 2) { T = cfma(lam, T, uu[i]); PINC(T); uu[i] = T; }
      else { hs[0][m / 3] = pk2(uu[i - 2].r, uu[i - 1].r); hs[1][m / 3] = pk2(uu[i - 2].i, uu[i - 1].i); DUE(hs[0][m / 3]); DUE(hs[1][m / 3]); }
    }
    if constexpr (K == K_PK || K == K_PK + 1) {
      const f32x4 &a = tm[2 * (K - K_PK)], &b = tm[2 * (K - K_PK) + 1];
      sf[K - K_PK] = u32x4{pk2(a[0], a[1]), pk2(a[2], a[3]), pk2(b[0], b[1]), pk2(b[2], b[3])}; DUE(sf[K - K_PK]);
    }
    if constexpr (K >= K_AD && K < K_CA) FA[K - K_AD].set(fa_ ^ (u32)(32 * (K - K_AD)));
    if constexpr (K == K_STS) { af[U1_][0] = sf[0]; st16<2048>(l16o, sf[0], Hw); vm += 1; }
    if constexpr (K == K_STS + 1) { af[U1_][1] = sf[1]; st16<3072>(l16o, sf[1], Hw); vm += 1; mS = vm; }
    if constexpr (K == K_CA) CA.set(lds0 + (u32)((G & 1) * 8 + ch * 128 + (((2 * kq + hk) ^ (ch >> 1)) * 16)));
    if constexpr (K == K_EA) { go = (u32)(64 * rt + 32 * th + 8 * G + (ch & 7)) * 8; PIN(go); }
    if constexpr (K == K_EA + 1) {
      eo[1] = (u32)(128 * nt + 8 * ch); eo[0] = (u32)(64 * rt + 32 * th + 8 * kq + G) * XROW + eo[1];
      PIN(eo[0]); PIN(eo[1]);
    }
    if constexpr (K == K_GG) {
      asm volatile("s_waitcnt vmcnt(%4)" : "+v"(gg), "+v"(xv[0]), "+v"(xv[1]), "+v"(dv) : "n"(vm - mE < 63 ? vm - mE : 63) : "memory");
      if (__builtin_expect(!all_lanes(gg[1] == tag), 0)) {
        u32 spins = 0;
        do {
          __builtin_amdgcn_s_sleep(1);
          asm volatile("global_load_dwordx2 %0, %1, %2 sc1\n\ts_waitcnt vmcnt(0)" : "+v"(gg) : "v"(go), "s"(ws + WS_G) : "memory");
        } while (!all_lanes(gg[1] == tag) && ++spins < SPIN_CAP);
      }
      gl = u2f(gg[0]);
      gs[0] = bperm((16 * kq + G) * 4, gl); gs[1] = bperm((16 * kq + 4 + G) * 4, gl);
      g2x = gl + gl; gl = 1.f - gl; PIN(g2x); PIN(gl);
    }
    if constexpr (K >= K_SK && K < K_SK + 4) {
      constexpr int n = K - K_SK, s = n >> 1, h = n & 1;
      sk[2 * n] = bflo(xv[s][h]) * bflo(dv[h]); sk[2 * n + 1] = bfhi(xv[s][h]) * bfhi(dv[h]);
      DUE(sk[2 * n]); DUE(sk[2 * n + 1]);
    }
    if constexpr (K >= K_SC && K < K_FW) {
      constexpr int i = K - K_SC;
      sfor<0, 4>([&](auto r_) AINL {
        float v = aS[i][r_.v];
        const float g = g2x;
        asm volatile("v_mul_f32_dpp %0, %1, %0 row_newbcast:%2 row_mask:0xf bank_mask:0xf" : "+v"(v) : "v"(g), "n"(4 * (i & 1) + r_.v));
        aS[i][r_.v] = v;
      });
    }
    if constexpr (K == K_RA) {
      PIN(gs[0]); PIN(gs[1]);
      rv = lds0 + (u32)(RED + th * 4 * RW + kq * 64 + ((G & 1) + 2 * (ch >> 3)) * 2112 + ((ch >> 2) & 1) * 1024 + hk * 512 + (ch & 3) * 16); PIN(rv);
    }
    if constexpr (K >= K_FW && K < K_LOC) {
      constexpr int i = K - K_FW;
      if constexpr (i == 0) DUE(wf[(NS - 1) % (PD + 1)]);
      sfor<0, 4>([&](auto r_) AINL {
        constexpr int r = r_.v;
        float o = aS[i][r];
        const float g = gl, f = aF[i][r];
        asm volatile("v_fmac_f32_dpp %0, %1, %2 row_newbcast:%3 row_mask:0xf bank_mask:0xf\n\tds_write_addtid_b32 %0 offset:%4"
                     : "+v"(o) : "v"(g), "v"(f), "n"(4 * (i & 1) + r), "n"(RED + ((r & 1) + 2 * (i >> 2)) * 2112 + ((i >> 1) & 1) * 1024 + (r >> 1) * 512 + (i & 1) * 256) : "memory");
      });
      SCHED();
    }
  };
  sfor<0, NS>([&](auto t_) AINL {
    constexpr int t = t_.v, j = (t + PD) / 8;
    if constexpr (is_bar(t)) {
      if constexpr (t == T_RD || t == T_RD2) asm volatile("s_waitcnt lgkmcnt(%0)" :: "n"(2 * (t - SC.of[t == T_RD ? K_E1 : K_TW] - 1)) : "memory");
      constexpr int un = j < 14 ? 0 : u_need(t);
      int need = j < 14 ? mW[j < 14 ? 2 * j + 1 : 0] : mU[un];
      if constexpr (un > 8) need = need > mU[8] ? need : mU[8];
      if constexpr (t == T_F0) need = need > mH0 ? need : mH0;
      if constexpr (t == T_F1) need = need > mS ? need : mS;
      if constexpr (t == T_F2) need = need > mH1 ? need : mH1;
      WV(vm - need < 63 ? vm - need : 63);
      BAR();
      if constexpr (t == T_F0) put_flag(IC<0>{});
      if constexpr (t == T_F1) put_flag(IC<1>{});
      if constexpr (t == T_F2) put_flag(IC<2>{});
    }
    SCHED();
    auto mm = [&](auto h_) AINL {
      constexpr int h = h_.v;
      if constexpr (t < 112) mfma<(t % 28 < 2)>(acc[t / 28][2 * h + (t & 1)], wf[t % (PD + 1)], xr[(t % 28 & ~1) + h]);
      else {
        constexpr int u = s_u(t);
        if constexpr (t == first_use(u)) WV(vm - mA[2 * u + h]);
        mfma<(u == 0 || u == 7)>((kind(u) == 1 ? aS : aF)[2 * s_c(t) + h], af[u][h], u < NR ? uf[u < NR ? u : 0][s_c(t)] : wf[t % (PD + 1)]);
      }
      SCHED();
    };
    mm(IC<0>{});
    if constexpr (t + PD < NS && (t + PD < 112 || t + PD >= 112 + 4 * NR)) wf[(t + PD) % (PD + 1)] = rdf(t + PD);
    SCHED();
    sfor<0, NR>([&](auto i_) AINL {
      constexpr int i = i_.v, c = (t - t_r(i)) / RS;
      if constexpr (t >= t_r(i) && (t - t_r(i)) % RS == 0 && c < 4) {
        uf[i][c] = rdtr((LAS char*)(uintptr_t)(fa_ ^ (u32)(32 * c)) + uslot(i), 2048);
        SCHED();
      }
    });
    if constexpr (t == T_RD) {
#pragma unroll
      for (int i = 0; i < 3; ++i) wg[i] = f32x2{((LAS float*)(uintptr_t)tr)[32 * i], ((LAS float*)(uintptr_t)tr)[32 * i + 16]};
    }
    if constexpr (t == T_RD2) {
#pragma unroll
      for (int i = 0; i < 2; ++i) wg[i] = f32x2{((LAS float*)(uintptr_t)tw)[128 * i], ((LAS float*)(uintptr_t)tw)[128 * i + 64]};
    }
    sfor<0, NU>([&](auto n_) AINL { if constexpr (SC.at[t][n_.v] >= 0) { task(IC<SC.at[t][n_.v]>{}); SCHED(); } });
    constexpr int wp = w_piece(t), u = u_piece(t) / 2, h = u_piece(t) % 2;
    const char* sp; u32 sv;
    if constexpr (wp >= 0) { sp = w_src(wp); glds_m0<wslot(wp), (wp / 14) * 128>(wb1, sp); }
    if constexpr (h >= 0) {
      if constexpr (kind(u) != 1) { sp = pw2 + (cu[c_of(u)] * (256 * XROW) + kind(u) * 32 * XROW); sv = h ? fo1 : fo0; }
      else { sp = csrc + cu[c_of(u)] * 128; sv = h ? co1 : co0; }
      glds_m0<uslot(u) + h * 1024, 0>(wb2, sp);
    }
    SCHED();
    mm(IC<1>{});
    if constexpr (wp >= 0) { glds_go<(wp / 14) * 128>(wo, sp); vm += 1; mW[wp] = vm; }
    else if constexpr (h >= 0) { glds_go<0>(sv, sp); vm += 1; mU[u] = vm; }
    if constexpr (t == TLB) { asm volatile("global_load_dwordx4 %0, %1, %2 sc1" : "=v"(lb) : "v"(lbo), "s"(granp) : "memory"); vm += 1; mLB = vm; }
    if constexpr (SC.an[t] >= 0) issue_a(IC<SC.an[t] / 2>{}, IC<SC.an[t] % 2>{});
    if constexpr (t == TC0 - PL0) poll(IC<0>{});
    if constexpr (t == TC1 - PL1) poll(IC<1>{});
    if constexpr (t == TC2 - PL2) poll(IC<2>{});
    if constexpr (t == TC0) check(IC<0>{});
    if constexpr (t == TC1) check(IC<1>{});
    if constexpr (t == TC2) check(IC<2>{});
    if constexpr (t == TT) {
      oo = eo[0] * 2; PIN(oo);
      const int w_ = w; const u32 o = oo;
      asm volatile("s_cmp_lg_u32 %0, 0\n\ts_cbranch_scc1 1f\n\ts_add_u32 m0, %3, %4\n\ts_mov_b64 exec, 1\n\tglobal_load_lds_dword %1, %2\n\ts_mov_b64 exec, -1\n1:"
                   :: "s"(w_), "v"(o), "s"(pout), "s"(wb0), "n"(CT + 1024) : "memory", "scc");
    }
    if constexpr (t == TP1) __builtin_amdgcn_s_setprio(1);
    if constexpr (t == TPY || t == TPW) { const int c_ = PYC; asm volatile("s_cmp_eq_u32 %0, 0\n\ts_cbranch_scc1 1f\n\ts_setprio 1\n1:" :: "s"(c_) : "scc"); }
    if constexpr (t == TPZ) __builtin_amdgcn_s_setprio(0);
    if constexpr (t == 182) asm volatile("s_mov_b32 m0, %0" :: "s"(lds0 + (u32)(4 * th + kq) * RW) : "memory");
    if constexpr (t == TE0) { asm volatile("global_load_dwordx2 %0, %1, %2 sc1" : "=v"(gg) : "v"(go), "s"(ws + WS_G) : "memory"); vm += 1; mE = vm; }
    if constexpr (t == TE1) { asm volatile("global_load_dwordx2 %0, %1, %2" : "=v"(xv[0]) : "v"(eo[0]), "s"(px) : "memory"); vm += 1; mE = vm; }
    if constexpr (t == TE2) { asm volatile("global_load_dwordx2 %0, %1, %2 offset:%3" : "=v"(xv[1]) : "v"(eo[0]), "s"(px), "n"(4 * XROW) : "memory"); vm += 1; mE = vm; }
    if constexpr (t == TE3) { asm volatile("global_load_dwordx2 %0, %1, %2" : "=v"(dv) : "v"(eo[1]), "s"(pdsk) : "memory"); vm += 1; mE = vm; }
    SCHED();
  });
  const char* po = pout + 2 * (D * 4);
  PINS(po);
  __builtin_amdgcn_s_setprio(0);
  asm volatile("s_nop %0" :: "n"(NPE)); SCHED();
  sfor<K_FW, K_LOC>([&](auto K_) AINL { if constexpr (SC.of[K_.v] < 0) task(K_); });
  WAIT_L0(); BAR();
  f32x4 pv[2][4];
#pragma unroll
  for (int i = 0; i < 8; ++i) { pv[i >> 2][i & 3] = *(LAS f32x4*)((LAS char*)(uintptr_t)rv + (i >> 2) * 256 + (i & 3) * RW); if (ORD) SCHED(); }
  SCHED();
  sfor<0, 2>([&](auto s_) AINL {
    constexpr int s = s_.v;
    const f32x4 k = {sk[4 * s], sk[4 * s + 1], sk[4 * s + 2], sk[4 * s + 3]};
    const f32x4 v = ((k * gs[s] + pv[s][0]) + pv[s][1]) + (pv[s][2] + pv[s][3]);
    if constexpr (OSC == 1) st16<(4 * s - 2) * (D * 4)>(oo, __builtin_bit_cast(u32x4, v), po);
    else if constexpr (OSC == 0) asm volatile("global_store_dwordx4 %0, %1, %2 offset:%3\n\ts_nop 1" :: "v"(oo), "v"(v), "s"(po), "n"((4 * s - 2) * (D * 4)) : "memory");
    else if constexpr (OSC == 2) asm volatile("global_store_dwordx4 %0, %1, %2 offset:%3 nt\n\ts_nop 1" :: "v"(oo), "v"(v), "s"(po), "n"((4 * s - 2) * (D * 4)) : "memory");
    else asm volatile("global_store_dwordx4 %0, %1, %2 offset:%3 sc0 sc1\n\ts_nop 1" :: "v"(oo), "v"(v), "s"(po), "n"((4 * s - 2) * (D * 4)) : "memory");
  });
  asm volatile(".Lfz1:");
}

extern "C" void kernel_launch(void* const* d_in, const int* in_sizes, int n_in, void* d_out, int out_size, void* d_ws,
                              size_t ws_size, hipStream_t stream) {
  Params p;
  p.x = (const u16*)d_in[0]; p.lre = (const u16*)d_in[1]; p.lim = (const u16*)d_in[2];
  p.bre = (const u16*)d_in[3]; p.bim = (const u16*)d_in[4]; p.cre = (const u16*)d_in[5]; p.cim = (const u16*)d_in[6];
  p.ldt = (const u16*)d_in[7]; p.dsk = (const u16*)d_in[8];
  p.w1 = (const u16*)d_in[10]; p.w3 = (const u16*)d_in[11]; p.w2 = (const u16*)d_in[12]; p.gw = (const u16*)d_in[13];
  p.out = (float*)d_out; p.ws = (char*)d_ws; p.x2 = p.x;
  fused<<<256, 512, 0, stream>>>(p);
}
```
